```python
import torch

def _softmax(x, axis=-1):
    return torch.softmax(x, dim=axis)
def _gelu(x):
    return torch.nn.functional.gelu(x, approximate="tanh")
def _silu(x):
    return torch.nn.functional.silu(x)
def _sigmoid(x):
    return torch.sigmoid(x)
def _layernorm(x, axis=-1, eps=1e-5):
    m = x.mean(dim=axis, keepdim=True)
    v = x.var(dim=axis, keepdim=True, unbiased=False)
    return (x - m) / torch.sqrt(v + eps)
def _scatter_add(tgt, idx, src, axis=0):
    return tgt.clone().index_add_(axis, idx, src)
def _segment_sum(src, seg, n):
    out = torch.zeros((n,) + src.shape[1:], dtype=src.dtype)
    return out.index_add_(0, seg, src)
def _pad(x, pw):
    flat = tuple(v for pair in reversed(pw) for v in pair)
    return torch.nn.functional.pad(x, flat)

kernel_name = 'sequential_preLN__sliding_block_cached__glu_gated_swiglu__decode'

def setup_inputs():

    _w = (lambda a: (lambda t: t.to(torch.bfloat16) if t.is_floating_point() else t)(torch.as_tensor(a)))
    def _A_setup():
        B, Sq, D = 1, 1, 512
        Hq, Hkv, Sk, Dh = 32, 8, 65536, 128
        assert Hq % Hkv == 0, (Hq, Hkv)
        return {
            "x": _w((lambda *s: torch.randn(*s, dtype=torch.bfloat16) * 0.05)(B, Sq, D)),
            "k_cache": _w((lambda *s: torch.randn(*s, dtype=torch.bfloat16) * 1.6)(B, Hkv, Sk, Dh)),
            "v_cache": _w((lambda *s: torch.randn(*s, dtype=torch.bfloat16) * 0.8)(B, Hkv, Sk, Dh)),
            "wq": _w((lambda *s: torch.randn(*s, dtype=torch.bfloat16) * 1.6)(D, Hq * Dh) * (D ** -0.5)),
            "wo": _w((lambda *s: torch.randn(*s, dtype=torch.bfloat16) * 0.8)(Hq * Dh, D) * ((Hq * Dh) ** -0.5)),
        }
    _A_in = _A_setup()

    def _B_setup():
        B, S, D, F = 1, 1, 512, 1792
        return {
            "x": _w((lambda *s: torch.randn(*s, dtype=torch.bfloat16) * 0.05)(B, S, D)),
            "w1": _w((lambda *s: torch.randn(*s, dtype=torch.bfloat16) * 0.4)(D, F) * (2.0 / D) ** 0.5),
            "w3": _w((lambda *s: torch.randn(*s, dtype=torch.bfloat16) * 0.4)(D, F) * (2.0 / D) ** 0.5),
            "w2": _w((lambda *s: torch.randn(*s, dtype=torch.bfloat16) * 0.4)(F, D) * (2.0 / F) ** 0.5),
        }
    _B_in = _B_setup()

    return {
        "A_x": _A_in["x"],
        "A_k_cache": _A_in["k_cache"],
        "A_v_cache": _A_in["v_cache"],
        "A_wq": _A_in["wq"],
        "A_wo": _A_in["wo"],
        "B_x": _B_in["x"],
        "B_w1": _B_in["w1"],
        "B_w3": _B_in["w3"],
        "B_w2": _B_in["w2"],
    }

def reference(A_x, A_k_cache, A_v_cache, A_wq, A_wo, B_x, B_w1, B_w3, B_w2):
    def _A_ref(x, k_cache, v_cache, wq, wo):
        B, Sq, D = x.shape
        Hq, Dh = 32, 128
        Hkv, Sk = k_cache.shape[1], k_cache.shape[2]
        W = min(4096, max(256, Sk // 4))
        rep = Hq // Hkv
        q = (lambda x, *d: x.permute(*d))((x @ wq).reshape(B, Sq, Hq, Dh), 0, 2, 1, 3)
        k = (lambda x, n, axis: torch.repeat_interleave(x, n, dim=axis))(k_cache[:, :, Sk - W:, :], rep, axis=1)
        v = (lambda x, n, axis: torch.repeat_interleave(x, n, dim=axis))(v_cache[:, :, Sk - W:, :], rep, axis=1)
        scores = torch.einsum("bhqd,bhkd->bhqk", q, k) * (Dh ** -0.5)
        p = _softmax(scores, axis=-1)
        o = torch.einsum("bhqk,bhkd->bhqd", p, v)
        return (lambda x, *d: x.permute(*d))(o, 0, 2, 1, 3).reshape(B, Sq, Hq * Dh) @ wo

    def _B_ref(x, w1, w3, w2):
        gate = torch.einsum("bsd,df->bsf", x, w1)
        up = torch.einsum("bsd,df->bsf", x, w3)
        h = _silu(gate) * up
        out = torch.einsum("bsf,fd->bsd", h, w2)
        return out

    x = A_x
    h = x + _A_ref(_layernorm(x, axis=-1), A_k_cache, A_v_cache, A_wq, A_wo)
    h2 = h + _B_ref(_layernorm(h, axis=-1), B_w1, B_w3, B_w2)
    return h2
```

```cpp
#include <hip/hip_runtime.h>
#include <hip/hip_bf16.h>
#include <stdint.h>

typedef unsigned short u16;
typedef unsigned int u32;
typedef u32 u32x4 __attribute__((ext_vector_type(4)));

constexpr int D = 512, HQ = 32, HKV = 8, SK = 65536, DH = 128, W = 4096, F = 1792;
constexpr int NSPLIT = 32, KEYS = W / NSPLIT;

__device__ __forceinline__ float bf2f(u16 b) { return __uint_as_float((u32)b << 16); }
__device__ __forceinline__ float bflo(u32 w) { return __uint_as_float(w << 16); }
__device__ __forceinline__ float bfhi(u32 w) { return __uint_as_float(w & 0xffff0000u); }
__device__ __forceinline__ u16 f2bf(float f) { __hip_bfloat16 h = __float2bfloat16(f); return *reinterpret_cast<u16*>(&h); }
__device__ __forceinline__ float rbf(float f) { return bf2f(f2bf(f)); }

__device__ float block_sum(float v, float* red) {
  for (int o = 32; o >= 1; o >>= 1) v += __shfl_xor(v, o);
  int w = threadIdx.x >> 6, nw = blockDim.x >> 6;
  __syncthreads();
  if ((threadIdx.x & 63) == 0) red[w] = v;
  __syncthreads();
  float s = 0.f;
  for (int i = 0; i < nw; ++i) s += red[i];
  return s;
}

__device__ void ln512(const float* xin, float* xout, float* red) {
  float s = 0.f;
  for (int i = threadIdx.x; i < D; i += blockDim.x) s += xin[i];
  float mean = block_sum(s, red) * (1.0f / D);
  float s2 = 0.f;
  for (int i = threadIdx.x; i < D; i += blockDim.x) { float d = xin[i] - mean; s2 += d * d; }
  float var = block_sum(s2, red) * (1.0f / D);
  float mb = rbf(mean), vb = rbf(var);
  float den = rbf(sqrtf(rbf(vb + 1e-5f)));
  for (int i = threadIdx.x; i < D; i += blockDim.x) xout[i] = rbf(rbf(xin[i] - mb) / den);
  __syncthreads();
}

__global__ void k_q(const u16* x, const u16* wq, float* q) {
  __shared__ float xs[D], xn[D], red[16], part[4][64];
  for (int i = threadIdx.x; i < D; i += blockDim.x) xs[i] = bf2f(x[i]);
  __syncthreads();
  ln512(xs, xn, red);
  int col = blockIdx.x * 64 + (threadIdx.x & 63), w = threadIdx.x >> 6;
  float acc = 0.f;
  for (int k = w * 128; k < w * 128 + 128; ++k) acc += xn[k] * bf2f(wq[(size_t)k * 4096 + col]);
  part[w][threadIdx.x & 63] = acc;
  __syncthreads();
  if (threadIdx.x < 64) q[col] = rbf(part[0][threadIdx.x] + part[1][threadIdx.x] + part[2][threadIdx.x] + part[3][threadIdx.x]);
}

__global__ void k_attn(const float* q, const u16* kc, const u16* vc, float* po, float* pml) {
  __shared__ float qs[4][DH], sc[4][KEYS], ml[4][2];
  int s = blockIdx.x, g = blockIdx.y, t = threadIdx.x;
  for (int i = t; i < 512; i += 256) qs[i >> 7][i & 127] = q[g * 512 + i];
  __syncthreads();
  const u16* kb = kc + ((size_t)g * SK + (SK - W) + s * KEYS) * DH;
  const u16* vb = vc + ((size_t)g * SK + (SK - W) + s * KEYS) * DH;
  for (int idx = t; idx < 4 * KEYS; idx += 256) {
    int h = idx / KEYS, j = idx % KEYS;
    float acc = 0.f;
    for (int d = 0; d < DH; ++d) acc += qs[h][d] * bf2f(kb[(size_t)j * DH + d]);
    sc[h][j] = rbf(rbf(acc) * 0.08838834764831845f);
  }
  __syncthreads();
  int w = t >> 6, l = t & 63;
  {
    float m = fmaxf(sc[w][l], sc[w][l + 64]);
    for (int o = 32; o >= 1; o >>= 1) m = fmaxf(m, __shfl_xor(m, o));
    float p0 = __expf(sc[w][l] - m), p1 = __expf(sc[w][l + 64] - m);
    float sum = p0 + p1;
    for (int o = 32; o >= 1; o >>= 1) sum += __shfl_xor(sum, o);
    sc[w][l] = p0; sc[w][l + 64] = p1;
    if (l == 0) { ml[w][0] = m; ml[w][1] = sum; }
  }
  __syncthreads();
  for (int idx = t; idx < 512; idx += 256) {
    int h = idx >> 7, d = idx & 127;
    float acc = 0.f;
    for (int j = 0; j < KEYS; ++j) acc += sc[h][j] * bf2f(vb[(size_t)j * DH + d]);
    po[((size_t)(g * 4 + h) * NSPLIT + s) * DH + d] = acc;
  }
  if (t < 4) { pml[((g * 4 + t) * NSPLIT + s) * 2] = ml[t][0]; pml[((g * 4 + t) * NSPLIT + s) * 2 + 1] = ml[t][1]; }
}

__global__ void k_wo(const float* po, const float* pml, const u16* wo, float* hp) {
  __shared__ float o[DH];
  int h = blockIdx.x, t = threadIdx.x;
  if (t < DH) {
    float M = -INFINITY;
    for (int s = 0; s < NSPLIT; ++s) M = fmaxf(M, pml[(h * NSPLIT + s) * 2]);
    float L = 0.f, acc = 0.f;
    for (int s = 0; s < NSPLIT; ++s) {
      float wgt = __expf(pml[(h * NSPLIT + s) * 2] - M);
      L += wgt * pml[(h * NSPLIT + s) * 2 + 1];
      acc += wgt * po[((size_t)h * NSPLIT + s) * DH + t];
    }
    o[t] = rbf(acc / L);
  }
  __syncthreads();
  for (int c = t; c < D; c += 256) {
    float acc = 0.f;
    for (int d = 0; d < DH; ++d) acc += o[d] * bf2f(wo[(size_t)(h * DH + d) * D + c]);
    hp[h * D + c] = acc;
  }
}

__global__ void k_ffn1(const u16* x, const float* hp, const u16* w1, const u16* w3, float* hbuf, float* act) {
  __shared__ float hs[D], hn[D], red[16], pg[4][64], pu[4][64];
  int t = threadIdx.x;
  for (int c = t; c < D; c += 256) {
    float acc = 0.f;
    for (int h = 0; h < HQ; ++h) acc += hp[h * D + c];
    hs[c] = rbf(bf2f(x[c]) + rbf(acc));
    if (blockIdx.x == 0) hbuf[c] = hs[c];
  }
  __syncthreads();
  ln512(hs, hn, red);
  int col = blockIdx.x * 64 + (t & 63), w = t >> 6;
  float ag = 0.f, au = 0.f;
  for (int k = w * 128; k < w * 128 + 128; ++k) {
    ag += hn[k] * bf2f(w1[(size_t)k * F + col]);
    au += hn[k] * bf2f(w3[(size_t)k * F + col]);
  }
  pg[w][t & 63] = ag; pu[w][t & 63] = au;
  __syncthreads();
  if (t < 64) {
    float g = rbf(pg[0][t] + pg[1][t] + pg[2][t] + pg[3][t]);
    float u = rbf(pu[0][t] + pu[1][t] + pu[2][t] + pu[3][t]);
    float sl = rbf(g / (1.0f + __expf(-g)));
    act[col] = rbf(sl * u);
  }
}

__global__ void k_ffn2(const float* hbuf, const float* act, const u16* w2, u16* out) {
  __shared__ float part[4][64];
  int t = threadIdx.x, col = blockIdx.x * 64 + (t & 63), w = t >> 6;
  float acc = 0.f;
  for (int k = w * 448; k < w * 448 + 448; ++k) acc += act[k] * bf2f(w2[(size_t)k * D + col]);
  part[w][t & 63] = acc;
  __syncthreads();
  if (t < 64) out[col] = f2bf(hbuf[col] + rbf(part[0][t] + part[1][t] + part[2][t] + part[3][t]));
}

extern "C" void kernel_launch(void* const* d_in, const int* in_sizes, int n_in,
                              void* d_out, int out_size, void* d_ws, size_t ws_size,
                              hipStream_t stream) {
  const u16* x = (const u16*)d_in[0];
  const u16* kc = (const u16*)d_in[1];
  const u16* vc = (const u16*)d_in[2];
  const u16* wq = (const u16*)d_in[3];
  const u16* wo = (const u16*)d_in[4];
  const u16* w1 = (const u16*)d_in[6];
  const u16* w3 = (const u16*)d_in[7];
  const u16* w2 = (const u16*)d_in[8];
  float* ws = (float*)d_ws;
  float* q = ws;
  float* po = q + 4096;
  float* pml = po + 32 * 32 * 128;
  float* hp = pml + 2048;
  float* hbuf = hp + 32 * 512;
  float* act = hbuf + 512;
  k_q<<<64, 256, 0, stream>>>(x, wq, q);
  k_attn<<<dim3(NSPLIT, HKV), 256, 0, stream>>>(q, kc, vc, po, pml);
  k_wo<<<32, 256, 0, stream>>>(po, pml, wo, hp);
  k_ffn1<<<28, 256, 0, stream>>>(x, hp, w1, w3, hbuf, act);
  k_ffn2<<<8, 256, 0, stream>>>(hbuf, act, w2, (u16*)d_out);
}
```

```cpp
#include <hip/hip_runtime.h>
#include <hip/hip_bf16.h>
#include <stdint.h>

typedef unsigned short u16;
typedef unsigned int u32;
typedef unsigned long long u64;
typedef u32 u32x4 __attribute__((ext_vector_type(4)));
typedef u32 u32x2 __attribute__((ext_vector_type(2)));
typedef float f32x4 __attribute__((ext_vector_type(4)));
typedef float f32x2 __attribute__((ext_vector_type(2)));
typedef short s16x8 __attribute__((ext_vector_type(8)));
typedef short s16x4 __attribute__((ext_vector_type(4)));
typedef __bf16 bf16x2 __attribute__((ext_vector_type(2)));
#define GAS __attribute__((address_space(1)))
#define LAS __attribute__((address_space(3)))

constexpr int SK = 65536, WIN = 4096, FF = 1792;
constexpr u32 WS_EPOCH = 0;
constexpr u32 WS_FLAG = 4096;
constexpr u32 WS_QG = 8192;
constexpr u32 WS_LSE = WS_QG + 8 * 8192;
constexpr u32 WS_O2 = WS_LSE + 256 * 128;
constexpr u32 WS_H3 = WS_O2 + 256 * 1024;
constexpr u32 WS_H5 = WS_H3 + 8 * 16384;
constexpr u32 WS_END = WS_H5 + 16 * 112 * 128;
constexpr u32 WS_STAMP = 4u << 20;
constexpr u32 L_W = 0;
constexpr u32 L_INIT = 49152;
constexpr u32 L_QS = L_INIT;
constexpr u32 L_QP = L_QS + 1024;
constexpr u32 L_MM = L_QP + 1024;
constexpr u32 L_LL = L_MM + 64;
constexpr u32 L_OF = L_LL + 64;
constexpr u32 L_INIT_END = L_OF + 128;
constexpr u32 L_OP = L_INIT_END;
constexpr u32 L_WT = L_OP + 8192;
constexpr u32 L_RED = L_WT + 256;
constexpr u32 L_ST = L_RED + 4096;
constexpr u32 L_HB = L_ST + 64;
constexpr u32 L_END = L_HB + 1024;
constexpr u32 L_TOTAL = 84 * 1024;
static_assert(L_END <= L_TOTAL, "lds");
constexpr u32 SENT = 0xFFFFFFFFu;
constexpr u32 SPIN_CAP = 1u << 18;

__device__ __forceinline__ float bflo(u32 w) { return __uint_as_float(w << 16); }
__device__ __forceinline__ float bfhi(u32 w) { return __uint_as_float(w & 0xffff0000u); }
__device__ __forceinline__ u32 cvtpk(float lo, float hi) { return __builtin_bit_cast(u32, __builtin_convertvector(f32x2{lo, hi}, bf16x2)); }
__device__ __forceinline__ float rbf(float f) { return bflo(cvtpk(f, f)); }
__device__ __forceinline__ u32 nosent(u32 w) { return w < 0xFFFFFFFEu ? w : 0xFFFFFFFEu; }
__device__ __forceinline__ float dot2(u32 a, u32 b, float c) { return __builtin_amdgcn_fdot2_f32_bf16(__builtin_bit_cast(bf16x2, a), __builtin_bit_cast(bf16x2, b), c, false); }
__device__ __forceinline__ f32x4 mfma(u32x4 a, u32x4 b, f32x4 c) { return __builtin_amdgcn_mfma_f32_16x16x32_bf16(__builtin_bit_cast(s16x8, a), __builtin_bit_cast(s16x8, b), c, 0, 0, 0); }
template <int CTRL> __device__ __forceinline__ float dpp(float x) { return __builtin_bit_cast(float, __builtin_amdgcn_mov_dpp(__builtin_bit_cast(int, x), CTRL, 0xf, 0xf, true)); }
constexpr int XOR1 = 0xB1, XOR2 = 0x4E, XOR7 = 0x141, ROR8 = 0x128, ROR4 = 0x124;
__device__ __forceinline__ float sw32_add(float a, float b) { auto r = __builtin_amdgcn_permlane32_swap(__float_as_uint(a), __float_as_uint(b), false, false); return __uint_as_float(r[0]) + __uint_as_float(r[1]); }
__device__ __forceinline__ float sw16_add(float a, float b) { auto r = __builtin_amdgcn_permlane16_swap(__float_as_uint(a), __float_as_uint(b), false, false); return __uint_as_float(r[0]) + __uint_as_float(r[1]); }
__device__ __forceinline__ float sw32_max(float a) { auto r = __builtin_amdgcn_permlane32_swap(__float_as_uint(a), __float_as_uint(a), false, false); return fmaxf(__uint_as_float(r[0]), __uint_as_float(r[1])); }
__device__ __forceinline__ float sw16_max(float a) { auto r = __builtin_amdgcn_permlane16_swap(__float_as_uint(a), __float_as_uint(a), false, false); return fmaxf(__uint_as_float(r[0]), __uint_as_float(r[1])); }
__device__ __forceinline__ float row16_sum(float v) { v += dpp<XOR1>(v); v += dpp<XOR2>(v); v += dpp<ROR4>(v); v += dpp<ROR8>(v); return v; }
__device__ __forceinline__ float wave_sum(float v) { v = row16_sum(v); v = sw16_add(v, v); return sw32_add(v, v); }
__device__ __forceinline__ float treduce8(const float (&a)[8], int lane) {
  float t0 = sw32_add(a[0], a[4]), t1 = sw32_add(a[1], a[5]), t2 = sw32_add(a[2], a[6]), t3 = sw32_add(a[3], a[7]);
  float u0 = sw16_add(t0, t2), u1 = sw16_add(t1, t3);
  float v0 = u0 + dpp<ROR8>(u0), v1 = u1 + dpp<ROR8>(u1);
  return (lane & 8) ? v1 : v0;
}
__device__ __forceinline__ u32 permlo(u32 hi, u32 lo) { return __builtin_amdgcn_perm(hi, lo, 0x05040100u); }
__device__ __forceinline__ u32 permhi(u32 hi, u32 lo) { return __builtin_amdgcn_perm(hi, lo, 0x07060302u); }
__device__ __forceinline__ u32x4 ldnt(const GAS char* p) { return __builtin_nontemporal_load((const GAS u32x4*)p); }
__device__ __forceinline__ u32x2 trd(LAS char* p) { return __builtin_bit_cast(u32x2, __builtin_amdgcn_ds_read_tr16_b64_v4i16((LAS s16x4*)p)); }
__device__ __forceinline__ void st4(GAS char* b, u32 off, u32 v) { asm volatile("global_store_dword %0, %1, %2 sc1" :: "v"(off), "v"(v), "s"(b) : "memory"); }
__device__ __forceinline__ void st8(GAS char* b, u32 off, u32x2 v) { asm volatile("global_store_dwordx2 %0, %1, %2 sc1" :: "v"(off), "v"(v), "s"(b) : "memory"); }
__device__ __forceinline__ void st16(GAS char* b, u32 off, u32x4 v) { asm volatile("global_store_dwordx4 %0, %1, %2 sc1\n\ts_nop 1" :: "v"(off), "v"(v), "s"(b) : "memory"); }
__device__ __forceinline__ bool has_sent(u32x4 v) { return max(max(v.x, v.y), max(v.z, v.w)) == SENT; }
#define LDS_FENCE() asm volatile("s_waitcnt lgkmcnt(0)" ::: "memory")
#define BARRIER() do { asm volatile("s_waitcnt lgkmcnt(0)" ::: "memory"); __builtin_amdgcn_s_barrier(); asm volatile("" ::: "memory"); } while (0)

#define STAMP(k) do { } while (0)

__device__ __forceinline__ void gather2(const GAS char* b, u32 o0, u32 o1, u32x4& v0, u32x4& v1) {
  u32 spins = 0; bool bad;
  do {
    asm volatile("global_load_dwordx4 %0, %2, %4 sc1\n\tglobal_load_dwordx4 %1, %3, %4 sc1\n\ts_waitcnt vmcnt(0)"
                 : "=&v"(v0), "=&v"(v1) : "v"(o0), "v"(o1), "s"(b) : "memory");
    bad = has_sent(v0) | has_sent(v1);
  } while (bad && ++spins < SPIN_CAP);
}

#define GET_TAG() asm volatile("s_waitcnt vmcnt(0)\n\tv_readfirstlane_b32 %0, %1\n\ts_nop 0\n\ts_add_u32 %0, %0, 1" : "=s"(tag) : "v"(ep), "v"(codepf) : "memory", "scc")
#define QDELAY 12
constexpr int NTHR = 640;
constexpr u32 CODE_BYTES = 9216;
__global__ void __launch_bounds__(NTHR, 3)
fused_decode(const char* __restrict__ x_, const char* __restrict__ kc_, const char* __restrict__ vc_, const char* __restrict__ wq_,
             const char* __restrict__ wo_, const char* __restrict__ w1_, const char* __restrict__ w3_, const char* __restrict__ w2_,
             char* out_, char* ws_) {
  __shared__ __attribute__((aligned(16))) char smem[L_TOTAL];
  LAS char* L = (LAS char*)smem;
  STAMP(0);
  const int tid = threadIdx.x, lane = tid & 63, w = __builtin_amdgcn_readfirstlane(tid >> 6);
  u32 codepf;
  {
    u64 pc;
    asm volatile("s_getpc_b64 %0" : "=s"(pc));
    asm volatile("global_load_dword %0, %1, %2" : "=v"(codepf) : "v"(min((u32)tid * 16u, CODE_BYTES - 4u)), "s"(pc & ~255ull) : "memory");
  }
  const int b = blockIdx.x, g = b & 7, i = b >> 3;
  const int n = lane & 15, c = lane >> 4;
  const bool is_wo = i < 16;
  const int hp = (i >> 3) & 1, cb = i & 7;
  const int j16 = i - 16;
  const bool is_ffn = !is_wo && j16 < 14;
  const int u = 14 * g + j16;
  const int rc = 2 * g + (j16 - 14);

  *(volatile LAS u32*)(L + L_INIT + tid * 4) = SENT;
  const int ks = i >> 3, ch = lane & 7, rg = lane >> 3;
  const u32 row0 = 128 * ks + 32 * w + 4 * rg;
  u32 oq = row0 * 8192 + (g * 512 + (i & 7) * 64) * 2 + ch * 16, ox = lane * 16, oxr = (row0 & ~7u) * 2;
  u32 ok = n * 256 + c * 16, ov = c * 1024 + n * 16, zero = 0;
  u64 kvo = ((u64)(g * SK + (SK - WIN) + i * 128 + (w - 4) * 32)) * 256 + 2048;
  asm volatile("" : "+v"(oq), "+v"(ox), "+v"(oxr), "+v"(ok), "+v"(ov), "+v"(zero), "+s"(kvo));
  asm volatile("" : "+s"(x_), "+s"(kc_), "+s"(vc_), "+s"(wq_), "+s"(wo_), "+s"(w1_), "+s"(w3_), "+s"(w2_), "+s"(out_), "+s"(ws_));
  const GAS char* x = (const GAS char*)x_;
  GAS char* ws = (GAS char*)ws_;
  BARRIER();

  u32x4 r[16], q_[6];
  u32 ep;
  asm volatile(
      "s_cmp_gt_u32 %[w], 3\n\t"
      "s_cbranch_scc1 1f\n\t"
      "global_load_dwordx4 %[q4], %[ox], %[x]\n\t"
      "global_load_dwordx4 %[q5], %[oxr], %[x]\n\t"
      "global_load_dwordx4 %[q0], %[oq], %[wq] nt\n\t"
      "global_load_dwordx4 %[q1], %[oq], %[wq1] nt\n\t"
      "global_load_dwordx4 %[q2], %[oq], %[wq2] nt\n\t"
      "global_load_dwordx4 %[q3], %[oq], %[wq3] nt\n\t"
      "s_branch 2f\n"
      "1:\n\t"
      "s_cmp_gt_u32 %[w], 7\n\t"
      "s_cbranch_scc1 2f\n\t"
      "global_load_dwordx4 %[r0], %[ok], %[kb] offset:-2048 nt\n\t"
      "global_load_dwordx4 %[r1], %[ok], %[kb] offset:-1984 nt\n\t"
      "global_load_dwordx4 %[r2], %[ok], %[kb] offset:-1920 nt\n\t"
      "global_load_dwordx4 %[r3], %[ok], %[kb] offset:-1856 nt\n\t"
      "global_load_dwordx4 %[r4], %[ok], %[kb] offset:2048 nt\n\t"
      "global_load_dwordx4 %[r5], %[ok], %[kb] offset:2112 nt\n\t"
      "global_load_dwordx4 %[r6], %[ok], %[kb] offset:2176 nt\n\t"
      "global_load_dwordx4 %[r7], %[ok], %[kb] offset:2240 nt\n"
      "2:"
      : [r0] "=&v"(r[0]), [r1] "=&v"(r[1]), [r2] "=&v"(r[2]), [r3] "=&v"(r[3]), [r4] "=&v"(r[4]), [r5] "=&v"(r[5]), [r6] "=&v"(r[6]), [r7] "=&v"(r[7]),
        [q0] "=&v"(q_[0]), [q1] "=&v"(q_[1]), [q2] "=&v"(q_[2]), [q3] "=&v"(q_[3]), [q4] "=&v"(q_[4]), [q5] "=&v"(q_[5])
      : [w] "s"(w), [ox] "v"(ox), [oxr] "v"(oxr), [oq] "v"(oq), [ok] "v"(ok), [x] "s"(x_), [wq] "s"(wq_), [wq1] "s"(wq_ + 8192), [wq2] "s"(wq_ + 16384),
        [wq3] "s"(wq_ + 24576), [kb] "s"(kc_ + kvo)
      : "memory", "scc");
  asm volatile(
      "s_bitcmp1_b32 %[w], 2\n\t"
      "s_cbranch_scc0 1f\n\t"
      "global_load_dwordx4 %[r0], %[ov], %[vb] offset:-2048 nt\n\t"
      "global_load_dwordx4 %[r1], %[ov], %[vb] offset:-1792 nt\n\t"
      "global_load_dwordx4 %[r2], %[ov], %[vb] offset:-1536 nt\n\t"
      "global_load_dwordx4 %[r3], %[ov], %[vb] offset:-1280 nt\n\t"
      "global_load_dwordx4 %[r4], %[ov], %[vb] offset:2048 nt\n\t"
      "global_load_dwordx4 %[r5], %[ov], %[vb] offset:2304 nt\n\t"
      "global_load_dwordx4 %[r6], %[ov], %[vb] offset:2560 nt\n\t"
      "global_load_dwordx4 %[r7], %[ov], %[vb] offset:2816 nt\n\t"
      "s_branch 2f\n"
      "1:\n\t"
      "global_load_dword %[ep], %[z], %[ws] sc1\n"
      "2:"
      : [r0] "=&v"(r[8]), [r1] "=&v"(r[9]), [r2] "=&v"(r[10]), [r3] "=&v"(r[11]), [r4] "=&v"(r[12]), [r5] "=&v"(r[13]), [r6] "=&v"(r[14]), [r7] "=&v"(r[15]), [ep] "=&v"(ep)
      : [w] "s"(w), [ov] "v"(ov), [z] "v"(zero), [vb] "s"(vc_ + kvo), [ws] "s"(ws_)
      : "memory", "scc");
  const int wr = w;
  STAMP(1);

  u32 tag = 0;
  if (wr < 4) {
    asm volatile("s_waitcnt vmcnt(5)" : "+v"(q_[4]), "+v"(q_[5]) : "v"(codepf) : "memory");
    const u32x4 xa = q_[4];
    float xf[8] = {bflo(xa.x), bfhi(xa.x), bflo(xa.y), bfhi(xa.y), bflo(xa.z), bfhi(xa.z), bflo(xa.w), bfhi(xa.w)};
    const float sh = __builtin_bit_cast(float, __builtin_amdgcn_readfirstlane(__builtin_bit_cast(int, xf[0])));
    float s1 = 0.f, s2 = 0.f;
#pragma unroll
    for (int k = 0; k < 8; ++k) { const float d = xf[k] - sh; s1 += d; s2 = fmaf(d, d, s2); }
    s1 = wave_sum(s1) * (1.0f / 512); s2 = wave_sum(s2) * (1.0f / 512);
    const float mb = rbf(sh + s1), var = fmaxf(fmaf(-s1, s1, s2), 0.f);
    const float rden = __builtin_amdgcn_rcpf(rbf(__builtin_amdgcn_sqrtf(rbf(rbf(var) + 1e-5f))));
    const u32 xr0 = (rg & 1) ? q_[5].z : q_[5].x, xr1 = (rg & 1) ? q_[5].w : q_[5].y;
    u32 p01 = cvtpk(rbf(bflo(xr0) - mb) * rden, rbf(bfhi(xr0) - mb) * rden);
    u32 p23 = cvtpk(rbf(bflo(xr1) - mb) * rden, rbf(bfhi(xr1) - mb) * rden);
    asm volatile("s_waitcnt vmcnt(1)" : "+v"(q_[0]), "+v"(q_[1]), "+v"(q_[2]), "+v"(q_[3]), "+v"(p01), "+v"(p23) :: "memory");
    float acc[8];
#pragma unroll
    for (int d = 0; d < 4; ++d) {
      acc[2 * d] = dot2(permlo(q_[3][d], q_[2][d]), p23, dot2(permlo(q_[1][d], q_[0][d]), p01, 0.f));
      acc[2 * d + 1] = dot2(permhi(q_[3][d], q_[2][d]), p23, dot2(permhi(q_[1][d], q_[0][d]), p01, 0.f));
    }
    const float qp = treduce8(acc, lane);
    *(volatile LAS u32*)(L + L_QP + w * 256 + (ch * 8 + rg) * 4) = nosent(__float_as_uint(qp));
    GET_TAG();
    STAMP(2);
    if (w == 1) {
      u32x2 a0, a1, a2, a3; u32 spins = 0;
      const u32 o = L_QP + (lane & 31) * 8;
      do {
        a0 = *(volatile LAS u32x2*)(L + o); a1 = *(volatile LAS u32x2*)(L + o + 256);
        a2 = *(volatile LAS u32x2*)(L + o + 512); a3 = *(volatile LAS u32x2*)(L + o + 768);
      } while ((has_sent(u32x4{a0.x, a0.y, a1.x, a1.y}) | has_sent(u32x4{a2.x, a2.y, a3.x, a3.y})) && ++spins < SPIN_CAP);
      const f32x2 s = (__builtin_bit_cast(f32x2, a0) + __builtin_bit_cast(f32x2, a1)) + (__builtin_bit_cast(f32x2, a2) + __builtin_bit_cast(f32x2, a3));
      if (lane < 32) st8(ws, WS_QG + g * 8192 + ks * 2048 + ((i & 7) * 32 + lane) * 8, u32x2{cvtpk(s.x, s.y), tag});
    }
    STAMP(3);
    __builtin_amdgcn_s_sleep(QDELAY);
    {
      const u32 o = WS_QG + g * 8192 + (lane >> 5) * 2048 + w * 512 + (lane & 31) * 16;
      u32x4 v0, v1; u32 spins = 0; bool bad;
      do {
        asm volatile("global_load_dwordx4 %0, %2, %4 sc1\n\tglobal_load_dwordx4 %1, %3, %4 sc1\n\ts_waitcnt vmcnt(0)"
                     : "=&v"(v0), "=&v"(v1) : "v"(o), "v"(o + 4096), "s"(ws) : "memory");
        bad = (((v0.y ^ tag) | (v0.w ^ tag)) | ((v1.y ^ tag) | (v1.w ^ tag))) != 0u;
      } while (bad && ++spins < SPIN_CAP);
      const float e0 = bflo(v0.x) + bflo(v1.x), e1 = bfhi(v0.x) + bfhi(v1.x), e2 = bflo(v0.z) + bflo(v1.z), e3 = bfhi(v0.z) + bfhi(v1.z);
      const float q0 = sw32_add(e0, e2), q1 = sw32_add(e1, e3);
      *(volatile LAS u32*)(L + L_QS + w * 256 + (lane & 31) * 8 + (lane >> 5) * 4) = nosent(cvtpk(q0, q1));
    }
    STAMP(4);
  } else if (wr < 8) {
    const int a = w - 4;
    u32x4 qf[4];
    {
      u32 spins = 0; bool bad;
      do {
#pragma unroll
        for (int s = 0; s < 4; ++s) qf[s] = *(volatile LAS u32x4*)(L + L_QS + (n >> 2) * 256 + s * 64 + c * 16);
        bad = has_sent(qf[0]) | has_sent(qf[1]) | has_sent(qf[2]) | has_sent(qf[3]);
      } while (__any(bad) && ++spins < SPIN_CAP);
    }
    STAMP(2);
    asm volatile("s_waitcnt vmcnt(8)" : "+v"(r[0]), "+v"(r[1]), "+v"(r[2]), "+v"(r[3]), "+v"(r[4]), "+v"(r[5]), "+v"(r[6]), "+v"(r[7]) : "v"(codepf) : "memory");
    f32x4 sc[2];
#pragma unroll
    for (int t = 0; t < 2; ++t) {
      sc[t] = f32x4{0.f, 0.f, 0.f, 0.f};
#pragma unroll
      for (int s = 0; s < 4; ++s) sc[t] = mfma(r[t * 4 + s], qf[s], sc[t]);
    }
    float sv[8];
#pragma unroll
    for (int k = 0; k < 4; ++k) {
      const u32 r1 = cvtpk(sc[k >> 1][(2 * k) & 3], sc[k >> 1][(2 * k + 1) & 3]);
      const u32 r2 = cvtpk(bflo(r1) * 0.08838834764831845f, bfhi(r1) * 0.08838834764831845f);
      sv[2 * k] = bflo(r2); sv[2 * k + 1] = bfhi(r2);
    }
    float m = fmaxf(fmaxf(fmaxf(sv[0], sv[1]), fmaxf(sv[2], sv[3])), fmaxf(fmaxf(sv[4], sv[5]), fmaxf(sv[6], sv[7])));
    m = sw32_max(sw16_max(m));
    if ((lane & 51) == 0) *(volatile LAS u32*)(L + L_MM + (n >> 2) * 16 + a * 4) = nosent(__float_as_uint(m));
    u32x4 mm;
    {
      u32 spins = 0;
      do { mm = *(volatile LAS u32x4*)(L + L_MM + (n >> 2) * 16); } while (__any(has_sent(mm)) && ++spins < SPIN_CAP);
    }
    const float M = fmaxf(fmaxf(__uint_as_float(mm.x), __uint_as_float(mm.y)), fmaxf(__uint_as_float(mm.z), __uint_as_float(mm.w)));
    float p[8], l = 0.f;
#pragma unroll
    for (int k = 0; k < 8; ++k) { p[k] = __builtin_amdgcn_exp2f((sv[k] - M) * 1.4426950408889634f); l += p[k]; }
    l = sw16_add(l, l); l = sw32_add(l, l);
    if ((lane & 51) == 0) *(volatile LAS u32*)(L + L_LL + (n >> 2) * 16 + a * 4) = nosent(__float_as_uint(l));
    const u32x4 pf = {cvtpk(p[0], p[1]), cvtpk(p[2], p[3]), cvtpk(p[4], p[5]), cvtpk(p[6], p[7])};
    STAMP(3);
    u32x4 bl[4], bh[4];
    __builtin_amdgcn_sched_barrier(0);
    asm volatile("s_waitcnt vmcnt(6)" : "+v"(r[8]), "+v"(r[9]) :: "memory");
#pragma unroll
    for (int d = 0; d < 4; ++d) { bl[d][0] = permlo(r[9][d], r[8][d]); bh[d][0] = permhi(r[9][d], r[8][d]); }
    __builtin_amdgcn_sched_barrier(0);
    asm volatile("s_waitcnt vmcnt(4)" : "+v"(r[10]), "+v"(r[11]) :: "memory");
#pragma unroll
    for (int d = 0; d < 4; ++d) { bl[d][1] = permlo(r[11][d], r[10][d]); bh[d][1] = permhi(r[11][d], r[10][d]); }
    __builtin_amdgcn_sched_barrier(0);
    asm volatile("s_waitcnt vmcnt(2)" : "+v"(r[12]), "+v"(r[13]) :: "memory");
#pragma unroll
    for (int d = 0; d < 4; ++d) { bl[d][2] = permlo(r[13][d], r[12][d]); bh[d][2] = permhi(r[13][d], r[12][d]); }
    __builtin_amdgcn_sched_barrier(0);
    asm volatile("s_waitcnt vmcnt(0)" : "+v"(r[14]), "+v"(r[15]) :: "memory");
    float o[8];
#pragma unroll
    for (int d = 0; d < 4; ++d) {
      bl[d][3] = permlo(r[15][d], r[14][d]); bh[d][3] = permhi(r[15][d], r[14][d]);
      o[2 * d] = mfma(pf, bl[d], f32x4{0.f, 0.f, 0.f, 0.f})[0];
      o[2 * d + 1] = mfma(pf, bh[d], f32x4{0.f, 0.f, 0.f, 0.f})[0];
    }
    *(volatile LAS f32x4*)(L + L_OP + a * 2048 + lane * 16) = f32x4{o[0], o[1], o[2], o[3]};
    *(volatile LAS f32x4*)(L + L_OP + a * 2048 + 1024 + lane * 16) = f32x4{o[4], o[5], o[6], o[7]};
    if (lane == 0) *(volatile LAS u32*)(L + L_OF + a * 4) = 1u;
    STAMP(4);
    {
      const u32 ro = L_OP + ((lane >> 1) & 1) * 1024 + (a * 16 + (lane >> 2)) * 16 + (lane & 1) * 8;
      u32x4 fl; u32 cl; f32x4 ll; f32x2 r0, r1, r2, r3; u32 spins = 0;
      do {
        fl = *(volatile LAS u32x4*)(L + L_OF);
        cl = *(volatile LAS u32*)(L + L_OF + 16);
        ll = *(volatile LAS f32x4*)(L + L_LL + a * 16);
        r0 = *(volatile LAS f32x2*)(L + ro); r1 = *(volatile LAS f32x2*)(L + ro + 2048);
        r2 = *(volatile LAS f32x2*)(L + ro + 4096); r3 = *(volatile LAS f32x2*)(L + ro + 6144);
      } while (__any(has_sent(fl) | (cl == SENT)) && ++spins < SPIN_CAP);
      const f32x2 s = ((r0 + r1) + (r2 + r3)) * __builtin_amdgcn_rcpf((ll[0] + ll[1]) + (ll[2] + ll[3]));
      st4(ws, WS_O2 + (g * 32 + i) * 1024 + a * 256 + lane * 4, nosent(cvtpk(s.x, s.y)));
    }
    STAMP(5);
  } else if (wr == 8) {
    {
      const u32x4 s4 = {SENT, SENT, SENT, SENT};
      st16(ws, WS_O2 + (g * 32 + i) * 1024 + lane * 16, s4);
      if (lane == 0) st16(ws, WS_LSE + (g * 32 + i) * 128, s4);
      if (is_wo) st16(ws, WS_H3 + rg * 16384 + (g * 2 + hp) * 1024 + cb * 128 + ch * 16, s4);
      if (is_ffn) {
        st16(ws, WS_H5 + (rg * 112 + u) * 128 + ch * 16, s4);
        st16(ws, WS_H5 + ((rg + 8) * 112 + u) * 128 + ch * 16, s4);
      }
      GET_TAG();
      if (lane == 0) { st8(ws, WS_FLAG + b * 8, u32x2{tag, ~tag}); *(volatile LAS u32*)(L + L_OF + 16) = 1u; *(volatile LAS u32*)(L + L_OF + 20) = tag; }
    }
    STAMP(2);
    {
      u32x4 mm, ll; u32 spins = 0;
      do { __builtin_amdgcn_s_sleep(1); mm = *(volatile LAS u32x4*)(L + L_MM + (lane & 3) * 16); ll = *(volatile LAS u32x4*)(L + L_LL + (lane & 3) * 16); } while (__any(has_sent(ll) | has_sent(mm)) && ++spins < SPIN_CAP);
      const float M = fmaxf(fmaxf(__uint_as_float(mm.x), __uint_as_float(mm.y)), fmaxf(__uint_as_float(mm.z), __uint_as_float(mm.w)));
      const float Ls = (__uint_as_float(ll.x) + __uint_as_float(ll.y)) + (__uint_as_float(ll.z) + __uint_as_float(ll.w));
      if (lane < 4) st4(ws, WS_LSE + (g * 32 + i) * 128 + lane * 4, nosent(__float_as_uint(fmaf(M, 1.4426950408889634f, __builtin_amdgcn_logf(Ls)))));
    }
    STAMP(3);
    {
      u32x4 v0, v1; u32 spins = 0; bool bad;
      do {
        asm volatile("global_load_dwordx4 %0, %2, %3 sc1\n\tglobal_load_dwordx4 %1, %2, %3 offset:1024 sc1\n\ts_waitcnt vmcnt(0)"
                     : "=&v"(v0), "=&v"(v1) : "v"(WS_FLAG + lane * 16), "s"(ws) : "memory");
        bad = (((v0.x ^ tag) | (v0.z ^ tag)) | ((v1.x ^ tag) | (v1.z ^ tag)) | ((v0.y ^ ~tag) | (v0.w ^ ~tag)) | ((v1.y ^ ~tag) | (v1.w ^ ~tag))) != 0u;
      } while (bad && ++spins < SPIN_CAP);
    }
    STAMP(4);
    if (is_wo) {
      u32 lv, spins = 0;
      do {
        asm volatile("global_load_dword %0, %1, %2 sc1\n\ts_waitcnt vmcnt(0)" : "=v"(lv) : "v"(WS_LSE + (g * 32 + (lane & 31)) * 128 + (hp * 2 + (lane >> 5)) * 4), "s"(ws) : "memory");
      } while (lv == SENT && ++spins < SPIN_CAP);
      const float ls = __uint_as_float(lv);
      float m = fmaxf(ls, dpp<XOR1>(ls)); m = fmaxf(m, dpp<XOR2>(m)); m = fmaxf(m, dpp<ROR4>(m)); m = fmaxf(m, dpp<ROR8>(m)); m = sw16_max(m);
      const float e = __builtin_amdgcn_exp2f(ls - m);
      float s = row16_sum(e); s = sw16_add(s, s);
      *(LAS float*)(L + L_WT + lane * 4) = e * __builtin_amdgcn_rcpf(s);
    }
    STAMP(5);
  } else {
    GET_TAG();
    {
      u32x4 q4; u32 spins = 0;
      do { __builtin_amdgcn_s_sleep(1); q4 = *(volatile LAS u32x4*)(L + L_QS + lane * 16); } while (__any(has_sent(q4)) && ++spins < SPIN_CAP);
    }
    STAMP(2);
    if (is_wo) {
      const GAS char* src = (const GAS char*)wo_ + (size_t)(g * 512 + hp * 256 + rg) * 1024 + cb * 128 + ch * 16;
#pragma unroll 1
      for (int k = 0; k < 32; ++k) __builtin_amdgcn_global_load_lds((const GAS u32*)(src + k * 8192), (LAS u32*)(L + L_W + k * 1024), 16, 0, 2);
    } else if (is_ffn) {
      const size_t o = (size_t)(lane >> 1) * (FF * 2) + u * 32 + (lane & 1) * 16;
#pragma unroll 1
      for (int k = 0; k < 16; ++k) __builtin_amdgcn_global_load_lds((const GAS u32*)((const GAS char*)w1_ + o + (size_t)k * 32 * FF * 2), (LAS u32*)(L + L_W + k * 1024), 16, 0, 2);
#pragma unroll 1
      for (int k = 0; k < 16; ++k) __builtin_amdgcn_global_load_lds((const GAS u32*)((const GAS char*)w3_ + o + (size_t)k * 32 * FF * 2), (LAS u32*)(L + L_W + 16384 + k * 1024), 16, 0, 2);
#pragma unroll 1
      for (int k = 0; k < 16; ++k) __builtin_amdgcn_global_load_lds((const GAS u32*)((const GAS char*)w2_ + (size_t)(u * 16 + k) * 1024 + lane * 16), (LAS u32*)(L + L_W + 32768 + k * 1024), 16, 0, 2);
    }
    STAMP(3);
    __builtin_amdgcn_s_waitcnt(0x0F70);
    STAMP(4);
  }
  BARRIER();
  STAMP(6);
  if (w >= 8) {
  } else if (is_wo) {
    u32x4 bf[4];
#pragma unroll
    for (int ct = 0; ct < 4; ++ct) {
      LAS char* p = L + L_W + (32 * w + 8 * c + (n >> 2)) * 128 + ct * 32 + (n & 3) * 8;
      const u32x2 lo = trd(p), hi = trd(p + 512);
      bf[ct] = u32x4{lo.x, lo.y, hi.x, hi.y};
    }
    const f32x4 wt0 = *(LAS f32x4*)(L + L_WT + (w >> 2) * 128 + c * 16), wt1 = *(LAS f32x4*)(L + L_WT + (w >> 2) * 128 + 64 + c * 16);
    u32x4 a0, a1;
    const u32 o = WS_O2 + (g * 32 + n) * 1024 + hp * 512 + w * 64 + c * 16;
    gather2(ws, o, o + 16384, a0, a1);
    STAMP(7);
    float y[4];
#pragma unroll
    for (int ct = 0; ct < 4; ++ct) {
      const f32x4 d0 = mfma(a0, bf[ct], f32x4{0.f, 0.f, 0.f, 0.f}), d1 = mfma(a1, bf[ct], f32x4{0.f, 0.f, 0.f, 0.f});
      const f32x4 e = d0 * wt0 + d1 * wt1;
      y[ct] = (e[0] + e[1]) + (e[2] + e[3]);
    }
    const float z = sw16_add(sw32_add(y[0], y[2]), sw32_add(y[1], y[3]));
    *(LAS float*)(L + L_RED + w * 256 + lane * 4) = z;
    BARRIER();
    if (w == 0) {
      f32x4 s0 = {0.f, 0.f, 0.f, 0.f}, s1 = s0;
#pragma unroll
      for (int k = 0; k < 8; ++k) { s0 += *(LAS f32x4*)(L + L_RED + k * 256 + (lane & 7) * 32); s1 += *(LAS f32x4*)(L + L_RED + k * 256 + (lane & 7) * 32 + 16); }
      st16(ws, WS_H3 + (lane >> 3) * 16384 + (g * 2 + hp) * 1024 + cb * 128 + (lane & 7) * 16,
           u32x4{nosent(cvtpk(s0[0], s0[1])), nosent(cvtpk(s0[2], s0[3])), nosent(cvtpk(s1[0], s1[1])), nosent(cvtpk(s1[2], s1[3]))});
    }
    STAMP(8);
  } else if (is_ffn) {
    u32x4 b1[2], b3[2], w2a, w2b;
#pragma unroll
    for (int s = 0; s < 2; ++s) {
      LAS char* p = L + L_W + (64 * w + 32 * s + 8 * c + (n >> 2)) * 32 + (n & 3) * 8;
      u32x2 lo = trd(p), hi = trd(p + 128);
      b1[s] = u32x4{lo.x, lo.y, hi.x, hi.y};
      lo = trd(p + 16384); hi = trd(p + 16384 + 128);
      b3[s] = u32x4{lo.x, lo.y, hi.x, hi.y};
    }
    {
      LAS char* p = L + L_W + 32768 + (n >> 2) * 1024 + (w * 64 + c * 16 + (n & 3) * 4) * 2;
      const u32x2 r0 = trd(p), r1 = trd(p + 4096), r2 = trd(p + 8192), r3 = trd(p + 12288);
      w2a = u32x4{r0.x, r0.y, r1.x, r1.y}; w2b = u32x4{r2.x, r2.y, r3.x, r3.y};
    }
    const int pg = rg, col = 64 * w + 8 * ch + pg;
    const float xv = __uint_as_float((u32)(*(const GAS u16*)(x + col * 2)) << 16);
    u32x4 a0, a1;
    const u32 o = WS_H3 + g * 16384 + pg * 1024 + w * 128 + ch * 16;
    gather2(ws, o, o + 8192, a0, a1);
    STAMP(7);
    float s[8];
#pragma unroll
    for (int d = 0; d < 4; ++d) { s[2 * d] = bflo(a0[d]) + bflo(a1[d]); s[2 * d + 1] = bfhi(a0[d]) + bfhi(a1[d]); }
    const float at = treduce8(s, lane);
    const u32 hb = cvtpk(xv + rbf(at), 0.f);
    *(LAS u16*)(L + L_HB + col * 2) = (u16)hb;
    const float h = bflo(hb);
    const float s1 = wave_sum(h), s2 = wave_sum(h * h);
    LDS_FENCE();
    u32x4 h0 = *(LAS u32x4*)(L + L_HB + w * 128 + c * 16), h1 = *(LAS u32x4*)(L + L_HB + w * 128 + 64 + c * 16);
    if (n & 1) { h0 = u32x4{0x3F803F80u, 0x3F803F80u, 0x3F803F80u, 0x3F803F80u}; h1 = h0; }
    const f32x4 dg = mfma(h1, b1[1], mfma(h0, b1[0], f32x4{0.f, 0.f, 0.f, 0.f}));
    const f32x4 du = mfma(h1, b3[1], mfma(h0, b3[0], f32x4{0.f, 0.f, 0.f, 0.f}));
    if (lane < 32) *(LAS f32x2*)(L + L_RED + w * 256 + lane * 8) = c == 0 ? f32x2{dg[0], dg[1]} : f32x2{du[0], du[1]};
    if (lane == 0) *(LAS f32x2*)(L + L_ST + w * 8) = f32x2{s1, s2};
    BARRIER();
    f32x2 r = {0.f, 0.f};
#pragma unroll
    for (int k = 0; k < 4; ++k) r += *(LAS f32x2*)(L + L_RED + ((lane >> 5) * 4 + k) * 256 + (lane & 31) * 8);
    f32x2 st = *(LAS f32x2*)(L + L_ST + (lane & 7) * 8);
    st.x += dpp<XOR1>(st.x); st.y += dpp<XOR1>(st.y); st.x += dpp<XOR2>(st.x); st.y += dpp<XOR2>(st.y); st.x += dpp<XOR7>(st.x); st.y += dpp<XOR7>(st.y);
    const float mean = st.x * (1.0f / 512), var = fmaxf(fmaf(-mean, mean, st.y * (1.0f / 512)), 0.f);
    const float mb = rbf(mean), rden = __builtin_amdgcn_rcpf(rbf(__builtin_amdgcn_sqrtf(rbf(rbf(var) + 1e-5f))));
    const float ac = sw32_add(r.x, r.x), cs = sw32_add(r.y, r.y);
    const float v = rbf(fmaf(-mb, cs, ac) * rden);
    auto gu = __builtin_amdgcn_permlane16_swap(__float_as_uint(v), __float_as_uint(v), false, false);
    const float gt = __uint_as_float(gu[0]), up = __uint_as_float(gu[1]);
    const float sl = rbf(gt * __builtin_amdgcn_rcpf(1.0f + __builtin_amdgcn_exp2f(gt * -1.4426950408889634f)));
    const float act = sl * up;
    const u32 ap = cvtpk(act, dpp<XOR1>(act));
    float y = 0.f;
#pragma unroll
    for (int k = 0; k < 4; ++k) y = dot2(w2a[k], __builtin_amdgcn_readlane(ap, 2 * k), y);
    float y2 = 0.f;
#pragma unroll
    for (int k = 0; k < 4; ++k) y2 = dot2(w2b[k], __builtin_amdgcn_readlane(ap, 8 + 2 * k), y2);
    st4(ws, WS_H5 + ((2 * w + (lane >> 5)) * 112 + u) * 128 + (lane & 31) * 4, nosent(__float_as_uint(y + y2)));
    STAMP(8);
  } else {
    float hv = 0.f;
    if (w == 7) {
      const int q4 = lane & 3, p = lane >> 2;
      u32x4 a0; u32 spins = 0;
      do {
        asm volatile("global_load_dwordx4 %0, %1, %2 sc1\n\ts_waitcnt vmcnt(0)" : "=v"(a0) : "v"(WS_H3 + g * 16384 + p * 1024 + rc * 64 + q4 * 16), "s"(ws) : "memory");
      } while (has_sent(a0) && ++spins < SPIN_CAP);
      float s[8];
#pragma unroll
      for (int d = 0; d < 4; ++d) { s[2 * d] = bflo(a0[d]); s[2 * d + 1] = bfhi(a0[d]); }
      float t[4], e[2];
#pragma unroll
      for (int k = 0; k < 4; ++k) t[k] = sw32_add(s[k], s[k + 4]);
      e[0] = sw16_add(t[0], t[2]); e[1] = sw16_add(t[1], t[3]);
      const float f0 = e[0] + dpp<ROR8>(e[0]), f1 = e[1] + dpp<ROR8>(e[1]);
      float at = (lane & 8) ? f1 : f0;
      at += __shfl_xor(at, 4);
      const int col = 8 * q4 + (lane >> 3);
      const float xv = __uint_as_float((u32)(*(const GAS u16*)(x + (32 * rc + col) * 2)) << 16);
      *(LAS float*)(L + L_RED + 7 * 128 + col * 4) = rbf(xv + rbf(at));
      STAMP(7);
    } else {
      u32x4 a0, a1;
      const u32 o = WS_H5 + rc * (112 * 128) + w * 2048 + lane * 16;
      gather2(ws, o, o + 1024, a0, a1);
      STAMP(7);
      const f32x4 s = __builtin_bit_cast(f32x4, a0) + __builtin_bit_cast(f32x4, a1);
      float z = sw16_add(sw32_add(s[0], s[2]), sw32_add(s[1], s[3]));
      z += dpp<ROR8>(z);
      *(LAS float*)(L + L_RED + w * 128 + ((lane & 7) * 4 + c) * 4) = z;
    }
    BARRIER();
    if (tid < 16) {
      f32x2 s = {0.f, 0.f};
#pragma unroll
      for (int k = 0; k < 7; ++k) s += *(LAS f32x2*)(L + L_RED + k * 128 + tid * 8);
      const f32x2 h = *(LAS f32x2*)(L + L_RED + 7 * 128 + tid * 8);
      *(GAS u32*)((GAS char*)out_ + rc * 64 + tid * 4) = cvtpk(h.x + rbf(s.x), h.y + rbf(s.y));
      if (rc == 0 && tid == 0) st4(ws, WS_EPOCH, tag);
    }
    (void)hv;
    STAMP(8);
  }
}

extern "C" void kernel_launch(void* const* d_in, const int* in_sizes, int n_in,
                              void* d_out, int out_size, void* d_ws, size_t ws_size,
                              hipStream_t stream) {
  fused_decode<<<256, NTHR, 0, stream>>>((const char*)d_in[0], (const char*)d_in[1], (const char*)d_in[2], (const char*)d_in[3],
                                        (const char*)d_in[4], (const char*)d_in[6], (const char*)d_in[7], (const char*)d_in[8],
                                        (char*)d_out, (char*)d_ws);
}
```

```cpp
#define STAMPS
#include <hip/hip_runtime.h>
#include <hip/hip_bf16.h>
#include <stdint.h>

typedef unsigned short u16;
typedef unsigned int u32;
typedef unsigned long long u64;
typedef u32 u32x4 __attribute__((ext_vector_type(4)));
typedef u32 u32x2 __attribute__((ext_vector_type(2)));
typedef float f32x4 __attribute__((ext_vector_type(4)));
typedef float f32x2 __attribute__((ext_vector_type(2)));
typedef short s16x8 __attribute__((ext_vector_type(8)));
typedef short s16x4 __attribute__((ext_vector_type(4)));
typedef __bf16 bf16x2 __attribute__((ext_vector_type(2)));
#define GAS __attribute__((address_space(1)))
#define LAS __attribute__((address_space(3)))

constexpr int SK = 65536, WIN = 4096, FF = 1792;
constexpr u32 WS_EPOCH = 0;
constexpr u32 WS_FLAG = 4096;
constexpr u32 WS_QG = 8192;
constexpr u32 WS_LSE = WS_QG + 8 * 8192;
constexpr u32 WS_O2 = WS_LSE + 256 * 128;
constexpr u32 WS_H3 = WS_O2 + 256 * 1024;
constexpr u32 WS_H5 = WS_H3 + 8 * 16384;
constexpr u32 WS_END = WS_H5 + 16 * 112 * 128;
constexpr u32 WS_STAMP = 4u << 20;
constexpr u32 L_W = 0;
constexpr u32 L_INIT = 49152;
constexpr u32 L_QS = L_INIT;
constexpr u32 L_QP = L_QS + 1024;
constexpr u32 L_MM = L_QP + 1024;
constexpr u32 L_LL = L_MM + 64;
constexpr u32 L_OF = L_LL + 64;
constexpr u32 L_INIT_END = L_OF + 128;
constexpr u32 L_OP = L_INIT_END;
constexpr u32 L_WT = L_OP + 8192;
constexpr u32 L_RED = L_WT + 256;
constexpr u32 L_ST = L_RED + 4096;
constexpr u32 L_HB = L_ST + 64;
constexpr u32 L_END = L_HB + 1024;
constexpr u32 L_TOTAL = 84 * 1024;
static_assert(L_END <= L_TOTAL, "lds");
constexpr u32 SENT = 0xFFFFFFFFu;
constexpr u32 SPIN_CAP = 1u << 18;

__device__ __forceinline__ float bflo(u32 w) { return __uint_as_float(w << 16); }
__device__ __forceinline__ float bfhi(u32 w) { return __uint_as_float(w & 0xffff0000u); }
__device__ __forceinline__ u32 cvtpk(float lo, float hi) { return __builtin_bit_cast(u32, __builtin_convertvector(f32x2{lo, hi}, bf16x2)); }
__device__ __forceinline__ float rbf(float f) { return bflo(cvtpk(f, f)); }
__device__ __forceinline__ u32 nosent(u32 w) { return w < 0xFFFFFFFEu ? w : 0xFFFFFFFEu; }
__device__ __forceinline__ float dot2(u32 a, u32 b, float c) { return __builtin_amdgcn_fdot2_f32_bf16(__builtin_bit_cast(bf16x2, a), __builtin_bit_cast(bf16x2, b), c, false); }
__device__ __forceinline__ f32x4 mfma(u32x4 a, u32x4 b, f32x4 c) { return __builtin_amdgcn_mfma_f32_16x16x32_bf16(__builtin_bit_cast(s16x8, a), __builtin_bit_cast(s16x8, b), c, 0, 0, 0); }
template <int CTRL> __device__ __forceinline__ float dpp(float x) { return __builtin_bit_cast(float, __builtin_amdgcn_mov_dpp(__builtin_bit_cast(int, x), CTRL, 0xf, 0xf, true)); }
constexpr int XOR1 = 0xB1, XOR2 = 0x4E, XOR7 = 0x141, ROR8 = 0x128, ROR4 = 0x124;
__device__ __forceinline__ float sw32_add(float a, float b) { auto r = __builtin_amdgcn_permlane32_swap(__float_as_uint(a), __float_as_uint(b), false, false); return __uint_as_float(r[0]) + __uint_as_float(r[1]); }
__device__ __forceinline__ float sw16_add(float a, float b) { auto r = __builtin_amdgcn_permlane16_swap(__float_as_uint(a), __float_as_uint(b), false, false); return __uint_as_float(r[0]) + __uint_as_float(r[1]); }
__device__ __forceinline__ float sw32_max(float a) { auto r = __builtin_amdgcn_permlane32_swap(__float_as_uint(a), __float_as_uint(a), false, false); return fmaxf(__uint_as_float(r[0]), __uint_as_float(r[1])); }
__device__ __forceinline__ float sw16_max(float a) { auto r = __builtin_amdgcn_permlane16_swap(__float_as_uint(a), __float_as_uint(a), false, false); return fmaxf(__uint_as_float(r[0]), __uint_as_float(r[1])); }
__device__ __forceinline__ float row16_sum(float v) { v += dpp<XOR1>(v); v += dpp<XOR2>(v); v += dpp<ROR4>(v); v += dpp<ROR8>(v); return v; }
__device__ __forceinline__ float wave_sum(float v) { v = row16_sum(v); v = sw16_add(v, v); return sw32_add(v, v); }
__device__ __forceinline__ float treduce8(const float (&a)[8], int lane) {
  float t0 = sw32_add(a[0], a[4]), t1 = sw32_add(a[1], a[5]), t2 = sw32_add(a[2], a[6]), t3 = sw32_add(a[3], a[7]);
  float u0 = sw16_add(t0, t2), u1 = sw16_add(t1, t3);
  float v0 = u0 + dpp<ROR8>(u0), v1 = u1 + dpp<ROR8>(u1);
  return (lane & 8) ? v1 : v0;
}
__device__ __forceinline__ u32 permlo(u32 hi, u32 lo) { return __builtin_amdgcn_perm(hi, lo, 0x05040100u); }
__device__ __forceinline__ u32 permhi(u32 hi, u32 lo) { return __builtin_amdgcn_perm(hi, lo, 0x07060302u); }
__device__ __forceinline__ u32x4 ldnt(const GAS char* p) { return __builtin_nontemporal_load((const GAS u32x4*)p); }
__device__ __forceinline__ u32x2 trd(LAS char* p) { return __builtin_bit_cast(u32x2, __builtin_amdgcn_ds_read_tr16_b64_v4i16((LAS s16x4*)p)); }
__device__ __forceinline__ void st4(GAS char* b, u32 off, u32 v) { asm volatile("global_store_dword %0, %1, %2 sc1" :: "v"(off), "v"(v), "s"(b) : "memory"); }
__device__ __forceinline__ void st8(GAS char* b, u32 off, u32x2 v) { asm volatile("global_store_dwordx2 %0, %1, %2 sc1" :: "v"(off), "v"(v), "s"(b) : "memory"); }
__device__ __forceinline__ void st16(GAS char* b, u32 off, u32x4 v) { asm volatile("global_store_dwordx4 %0, %1, %2 sc1\n\ts_nop 1" :: "v"(off), "v"(v), "s"(b) : "memory"); }
__device__ __forceinline__ bool has_sent(u32x4 v) { return max(max(v.x, v.y), max(v.z, v.w)) == SENT; }
#define LDS_FENCE() asm volatile("s_waitcnt lgkmcnt(0)" ::: "memory")
#define BARRIER() do { asm volatile("s_waitcnt lgkmcnt(0)" ::: "memory"); __builtin_amdgcn_s_barrier(); asm volatile("" ::: "memory"); } while (0)

#define STAMP(k) do { u64 t_; asm volatile("s_memrealtime %0\n\ts_waitcnt lgkmcnt(0)" : "=s"(t_) :: "memory"); ts[k] = t_; } while (0)

__device__ __forceinline__ void gather2(const GAS char* b, u32 o0, u32 o1, u32x4& v0, u32x4& v1) {
  u32 spins = 0; bool bad;
  do {
    asm volatile("global_load_dwordx4 %0, %2, %4 sc1\n\tglobal_load_dwordx4 %1, %3, %4 sc1\n\ts_waitcnt vmcnt(0)"
                 : "=&v"(v0), "=&v"(v1) : "v"(o0), "v"(o1), "s"(b) : "memory");
    bad = has_sent(v0) | has_sent(v1);
  } while (bad && ++spins < SPIN_CAP);
}

#define GET_TAG() asm volatile("s_waitcnt vmcnt(0)\n\tv_readfirstlane_b32 %0, %1\n\ts_nop 0\n\ts_add_u32 %0, %0, 1" : "=s"(tag) : "v"(ep), "v"(codepf) : "memory", "scc")
#define QDELAY 12
constexpr int NTHR = 640;
constexpr u32 CODE_BYTES = 9216;
__global__ void __launch_bounds__(NTHR, 3)
fused_decode(const char* __restrict__ x_, const char* __restrict__ kc_, const char* __restrict__ vc_, const char* __restrict__ wq_,
             const char* __restrict__ wo_, const char* __restrict__ w1_, const char* __restrict__ w3_, const char* __restrict__ w2_,
             char* out_, char* ws_) {
  __shared__ __attribute__((aligned(16))) char smem[L_TOTAL];
  LAS char* L = (LAS char*)smem;
  u64 ts[12];
  for (int k = 0; k < 12; ++k) ts[k] = 0;
  STAMP(0);
  const int tid = threadIdx.x, lane = tid & 63, w = __builtin_amdgcn_readfirstlane(tid >> 6);
  u32 codepf;
  {
    u64 pc;
    asm volatile("s_getpc_b64 %0" : "=s"(pc));
    asm volatile("global_load_dword %0, %1, %2" : "=v"(codepf) : "v"(min((u32)tid * 16u, CODE_BYTES - 4u)), "s"(pc & ~255ull) : "memory");
  }
  const int b = blockIdx.x, g = b & 7, i = b >> 3;
  const int n = lane & 15, c = lane >> 4;
  const bool is_wo = i < 16;
  const int hp = (i >> 3) & 1, cb = i & 7;
  const int j16 = i - 16;
  const bool is_ffn = !is_wo && j16 < 14;
  const int u = 14 * g + j16;
  const int rc = 2 * g + (j16 - 14);

  *(volatile LAS u32*)(L + L_INIT + tid * 4) = SENT;
  const int ks = i >> 3, ch = lane & 7, rg = lane >> 3;
  const u32 row0 = 128 * ks + 32 * w + 4 * rg;
  u32 oq = row0 * 8192 + (g * 512 + (i & 7) * 64) * 2 + ch * 16, ox = lane * 16, oxr = (row0 & ~7u) * 2;
  u32 ok = n * 256 + c * 16, ov = c * 1024 + n * 16, zero = 0;
  u64 kvo = ((u64)(g * SK + (SK - WIN) + i * 128 + (w - 4) * 32)) * 256 + 2048;
  asm volatile("" : "+v"(oq), "+v"(ox), "+v"(oxr), "+v"(ok), "+v"(ov), "+v"(zero), "+s"(kvo));
  asm volatile("" : "+s"(x_), "+s"(kc_), "+s"(vc_), "+s"(wq_), "+s"(wo_), "+s"(w1_), "+s"(w3_), "+s"(w2_), "+s"(out_), "+s"(ws_));
  const GAS char* x = (const GAS char*)x_;
  GAS char* ws = (GAS char*)ws_;
  BARRIER();

  u32x4 r[16], q_[6];
  u32 ep;
  asm volatile(
      "s_cmp_gt_u32 %[w], 3\n\t"
      "s_cbranch_scc1 1f\n\t"
      "global_load_dwordx4 %[q4], %[ox], %[x]\n\t"
      "global_load_dwordx4 %[q5], %[oxr], %[x]\n\t"
      "global_load_dwordx4 %[q0], %[oq], %[wq] nt\n\t"
      "global_load_dwordx4 %[q1], %[oq], %[wq1] nt\n\t"
      "global_load_dwordx4 %[q2], %[oq], %[wq2] nt\n\t"
      "global_load_dwordx4 %[q3], %[oq], %[wq3] nt\n\t"
      "s_branch 2f\n"
      "1:\n\t"
      "s_cmp_gt_u32 %[w], 7\n\t"
      "s_cbranch_scc1 2f\n\t"
      "global_load_dwordx4 %[r0], %[ok], %[kb] offset:-2048 nt\n\t"
      "global_load_dwordx4 %[r1], %[ok], %[kb] offset:-1984 nt\n\t"
      "global_load_dwordx4 %[r2], %[ok], %[kb] offset:-1920 nt\n\t"
      "global_load_dwordx4 %[r3], %[ok], %[kb] offset:-1856 nt\n\t"
      "global_load_dwordx4 %[r4], %[ok], %[kb] offset:2048 nt\n\t"
      "global_load_dwordx4 %[r5], %[ok], %[kb] offset:2112 nt\n\t"
      "global_load_dwordx4 %[r6], %[ok], %[kb] offset:2176 nt\n\t"
      "global_load_dwordx4 %[r7], %[ok], %[kb] offset:2240 nt\n"
      "2:"
      : [r0] "=&v"(r[0]), [r1] "=&v"(r[1]), [r2] "=&v"(r[2]), [r3] "=&v"(r[3]), [r4] "=&v"(r[4]), [r5] "=&v"(r[5]), [r6] "=&v"(r[6]), [r7] "=&v"(r[7]),
        [q0] "=&v"(q_[0]), [q1] "=&v"(q_[1]), [q2] "=&v"(q_[2]), [q3] "=&v"(q_[3]), [q4] "=&v"(q_[4]), [q5] "=&v"(q_[5])
      : [w] "s"(w), [ox] "v"(ox), [oxr] "v"(oxr), [oq] "v"(oq), [ok] "v"(ok), [x] "s"(x_), [wq] "s"(wq_), [wq1] "s"(wq_ + 8192), [wq2] "s"(wq_ + 16384),
        [wq3] "s"(wq_ + 24576), [kb] "s"(kc_ + kvo)
      : "memory", "scc");
  asm volatile(
      "s_bitcmp1_b32 %[w], 2\n\t"
      "s_cbranch_scc0 1f\n\t"
      "global_load_dwordx4 %[r0], %[ov], %[vb] offset:-2048 nt\n\t"
      "global_load_dwordx4 %[r1], %[ov], %[vb] offset:-1792 nt\n\t"
      "global_load_dwordx4 %[r2], %[ov], %[vb] offset:-1536 nt\n\t"
      "global_load_dwordx4 %[r3], %[ov], %[vb] offset:-1280 nt\n\t"
      "global_load_dwordx4 %[r4], %[ov], %[vb] offset:2048 nt\n\t"
      "global_load_dwordx4 %[r5], %[ov], %[vb] offset:2304 nt\n\t"
      "global_load_dwordx4 %[r6], %[ov], %[vb] offset:2560 nt\n\t"
      "global_load_dwordx4 %[r7], %[ov], %[vb] offset:2816 nt\n\t"
      "s_branch 2f\n"
      "1:\n\t"
      "global_load_dword %[ep], %[z], %[ws] sc1\n"
      "2:"
      : [r0] "=&v"(r[8]), [r1] "=&v"(r[9]), [r2] "=&v"(r[10]), [r3] "=&v"(r[11]), [r4] "=&v"(r[12]), [r5] "=&v"(r[13]), [r6] "=&v"(r[14]), [r7] "=&v"(r[15]), [ep] "=&v"(ep)
      : [w] "s"(w), [ov] "v"(ov), [z] "v"(zero), [vb] "s"(vc_ + kvo), [ws] "s"(ws_)
      : "memory", "scc");
  const int wr = w;
  STAMP(1);

  u32 tag = 0;
  if (wr < 4) {
    asm volatile("s_waitcnt vmcnt(5)" : "+v"(q_[4]), "+v"(q_[5]) : "v"(codepf) : "memory");
    const u32x4 xa = q_[4];
    float xf[8] = {bflo(xa.x), bfhi(xa.x), bflo(xa.y), bfhi(xa.y), bflo(xa.z), bfhi(xa.z), bflo(xa.w), bfhi(xa.w)};
    const float sh = __builtin_bit_cast(float, __builtin_amdgcn_readfirstlane(__builtin_bit_cast(int, xf[0])));
    float s1 = 0.f, s2 = 0.f;
#pragma unroll
    for (int k = 0; k < 8; ++k) { const float d = xf[k] - sh; s1 += d; s2 = fmaf(d, d, s2); }
    s1 = wave_sum(s1) * (1.0f / 512); s2 = wave_sum(s2) * (1.0f / 512);
    const float mb = rbf(sh + s1), var = fmaxf(fmaf(-s1, s1, s2), 0.f);
    const float rden = __builtin_amdgcn_rcpf(rbf(__builtin_amdgcn_sqrtf(rbf(rbf(var) + 1e-5f))));
    const u32 xr0 = (rg & 1) ? q_[5].z : q_[5].x, xr1 = (rg & 1) ? q_[5].w : q_[5].y;
    u32 p01 = cvtpk(rbf(bflo(xr0) - mb) * rden, rbf(bfhi(xr0) - mb) * rden);
    u32 p23 = cvtpk(rbf(bflo(xr1) - mb) * rden, rbf(bfhi(xr1) - mb) * rden);
    asm volatile("s_waitcnt vmcnt(1)" : "+v"(q_[0]), "+v"(q_[1]), "+v"(q_[2]), "+v"(q_[3]), "+v"(p01), "+v"(p23) :: "memory");
    float acc[8];
#pragma unroll
    for (int d = 0; d < 4; ++d) {
      acc[2 * d] = dot2(permlo(q_[3][d], q_[2][d]), p23, dot2(permlo(q_[1][d], q_[0][d]), p01, 0.f));
      acc[2 * d + 1] = dot2(permhi(q_[3][d], q_[2][d]), p23, dot2(permhi(q_[1][d], q_[0][d]), p01, 0.f));
    }
    const float qp = treduce8(acc, lane);
    *(volatile LAS u32*)(L + L_QP + w * 256 + (ch * 8 + rg) * 4) = nosent(__float_as_uint(qp));
    GET_TAG();
    STAMP(2);
    if (w == 1) {
      u32x2 a0, a1, a2, a3; u32 spins = 0;
      const u32 o = L_QP + (lane & 31) * 8;
      do {
        a0 = *(volatile LAS u32x2*)(L + o); a1 = *(volatile LAS u32x2*)(L + o + 256);
        a2 = *(volatile LAS u32x2*)(L + o + 512); a3 = *(volatile LAS u32x2*)(L + o + 768);
      } while ((has_sent(u32x4{a0.x, a0.y, a1.x, a1.y}) | has_sent(u32x4{a2.x, a2.y, a3.x, a3.y})) && ++spins < SPIN_CAP);
      const f32x2 s = (__builtin_bit_cast(f32x2, a0) + __builtin_bit_cast(f32x2, a1)) + (__builtin_bit_cast(f32x2, a2) + __builtin_bit_cast(f32x2, a3));
      if (lane < 32) st8(ws, WS_QG + g * 8192 + ks * 2048 + ((i & 7) * 32 + lane) * 8, u32x2{cvtpk(s.x, s.y), tag});
    }
    STAMP(3);
    __builtin_amdgcn_s_sleep(QDELAY);
    {
      const u32 o = WS_QG + g * 8192 + (lane >> 5) * 2048 + w * 512 + (lane & 31) * 16;
      u32x4 v0, v1; u32 spins = 0; bool bad;
      do {
        asm volatile("global_load_dwordx4 %0, %2, %4 sc1\n\tglobal_load_dwordx4 %1, %3, %4 sc1\n\ts_waitcnt vmcnt(0)"
                     : "=&v"(v0), "=&v"(v1) : "v"(o), "v"(o + 4096), "s"(ws) : "memory");
        bad = (((v0.y ^ tag) | (v0.w ^ tag)) | ((v1.y ^ tag) | (v1.w ^ tag))) != 0u;
        ts[10] += 1;
      } while (bad && ++spins < SPIN_CAP);
      const float e0 = bflo(v0.x) + bflo(v1.x), e1 = bfhi(v0.x) + bfhi(v1.x), e2 = bflo(v0.z) + bflo(v1.z), e3 = bfhi(v0.z) + bfhi(v1.z);
      const float q0 = sw32_add(e0, e2), q1 = sw32_add(e1, e3);
      *(volatile LAS u32*)(L + L_QS + w * 256 + (lane & 31) * 8 + (lane >> 5) * 4) = nosent(cvtpk(q0, q1));
    }
    STAMP(4);
  } else if (wr < 8) {
    const int a = w - 4;
    u32x4 qf[4];
    {
      u32 spins = 0; bool bad;
      do {
#pragma unroll
        for (int s = 0; s < 4; ++s) qf[s] = *(volatile LAS u32x4*)(L + L_QS + (n >> 2) * 256 + s * 64 + c * 16);
        bad = has_sent(qf[0]) | has_sent(qf[1]) | has_sent(qf[2]) | has_sent(qf[3]);
      } while (__any(bad) && ++spins < SPIN_CAP);
    }
    STAMP(2);
    asm volatile("s_waitcnt vmcnt(8)" : "+v"(r[0]), "+v"(r[1]), "+v"(r[2]), "+v"(r[3]), "+v"(r[4]), "+v"(r[5]), "+v"(r[6]), "+v"(r[7]) : "v"(codepf) : "memory");
    f32x4 sc[2];
#pragma unroll
    for (int t = 0; t < 2; ++t) {
      sc[t] = f32x4{0.f, 0.f, 0.f, 0.f};
#pragma unroll
      for (int s = 0; s < 4; ++s) sc[t] = mfma(r[t * 4 + s], qf[s], sc[t]);
    }
    float sv[8];
#pragma unroll
    for (int k = 0; k < 4; ++k) {
      const u32 r1 = cvtpk(sc[k >> 1][(2 * k) & 3], sc[k >> 1][(2 * k + 1) & 3]);
      const u32 r2 = cvtpk(bflo(r1) * 0.08838834764831845f, bfhi(r1) * 0.08838834764831845f);
      sv[2 * k] = bflo(r2); sv[2 * k + 1] = bfhi(r2);
    }
    float m = fmaxf(fmaxf(fmaxf(sv[0], sv[1]), fmaxf(sv[2], sv[3])), fmaxf(fmaxf(sv[4], sv[5]), fmaxf(sv[6], sv[7])));
    m = sw32_max(sw16_max(m));
    if ((lane & 51) == 0) *(volatile LAS u32*)(L + L_MM + (n >> 2) * 16 + a * 4) = nosent(__float_as_uint(m));
    u32x4 mm;
    {
      u32 spins = 0;
      do { mm = *(volatile LAS u32x4*)(L + L_MM + (n >> 2) * 16); } while (__any(has_sent(mm)) && ++spins < SPIN_CAP);
    }
    const float M = fmaxf(fmaxf(__uint_as_float(mm.x), __uint_as_float(mm.y)), fmaxf(__uint_as_float(mm.z), __uint_as_float(mm.w)));
    float p[8], l = 0.f;
#pragma unroll
    for (int k = 0; k < 8; ++k) { p[k] = __builtin_amdgcn_exp2f((sv[k] - M) * 1.4426950408889634f); l += p[k]; }
    l = sw16_add(l, l); l = sw32_add(l, l);
    if ((lane & 51) == 0) *(volatile LAS u32*)(L + L_LL + (n >> 2) * 16 + a * 4) = nosent(__float_as_uint(l));
    const u32x4 pf = {cvtpk(p[0], p[1]), cvtpk(p[2], p[3]), cvtpk(p[4], p[5]), cvtpk(p[6], p[7])};
    STAMP(3);
    u32x4 bl[4], bh[4];
    __builtin_amdgcn_sched_barrier(0);
    asm volatile("s_waitcnt vmcnt(6)" : "+v"(r[8]), "+v"(r[9]) :: "memory");
#pragma unroll
    for (int d = 0; d < 4; ++d) { bl[d][0] = permlo(r[9][d], r[8][d]); bh[d][0] = permhi(r[9][d], r[8][d]); }
    __builtin_amdgcn_sched_barrier(0);
    asm volatile("s_waitcnt vmcnt(4)" : "+v"(r[10]), "+v"(r[11]) :: "memory");
#pragma unroll
    for (int d = 0; d < 4; ++d) { bl[d][1] = permlo(r[11][d], r[10][d]); bh[d][1] = permhi(r[11][d], r[10][d]); }
    __builtin_amdgcn_sched_barrier(0);
    asm volatile("s_waitcnt vmcnt(2)" : "+v"(r[12]), "+v"(r[13]) :: "memory");
#pragma unroll
    for (int d = 0; d < 4; ++d) { bl[d][2] = permlo(r[13][d], r[12][d]); bh[d][2] = permhi(r[13][d], r[12][d]); }
    __builtin_amdgcn_sched_barrier(0);
    asm volatile("s_waitcnt vmcnt(0)" : "+v"(r[14]), "+v"(r[15]) :: "memory");
    float o[8];
#pragma unroll
    for (int d = 0; d < 4; ++d) {
      bl[d][3] = permlo(r[15][d], r[14][d]); bh[d][3] = permhi(r[15][d], r[14][d]);
      o[2 * d] = mfma(pf, bl[d], f32x4{0.f, 0.f, 0.f, 0.f})[0];
      o[2 * d + 1] = mfma(pf, bh[d], f32x4{0.f, 0.f, 0.f, 0.f})[0];
    }
    *(volatile LAS f32x4*)(L + L_OP + a * 2048 + lane * 16) = f32x4{o[0], o[1], o[2], o[3]};
    *(volatile LAS f32x4*)(L + L_OP + a * 2048 + 1024 + lane * 16) = f32x4{o[4], o[5], o[6], o[7]};
    if (lane == 0) *(volatile LAS u32*)(L + L_OF + a * 4) = 1u;
    STAMP(4);
    {
      const u32 ro = L_OP + ((lane >> 1) & 1) * 1024 + (a * 16 + (lane >> 2)) * 16 + (lane & 1) * 8;
      u32x4 fl; u32 cl; f32x4 ll; f32x2 r0, r1, r2, r3; u32 spins = 0;
      do {
        fl = *(volatile LAS u32x4*)(L + L_OF);
        cl = *(volatile LAS u32*)(L + L_OF + 16);
        ll = *(volatile LAS f32x4*)(L + L_LL + a * 16);
        r0 = *(volatile LAS f32x2*)(L + ro); r1 = *(volatile LAS f32x2*)(L + ro + 2048);
        r2 = *(volatile LAS f32x2*)(L + ro + 4096); r3 = *(volatile LAS f32x2*)(L + ro + 6144);
      } while (__any(has_sent(fl) | (cl == SENT)) && ++spins < SPIN_CAP);
      const f32x2 s = ((r0 + r1) + (r2 + r3)) * __builtin_amdgcn_rcpf((ll[0] + ll[1]) + (ll[2] + ll[3]));
      st4(ws, WS_O2 + (g * 32 + i) * 1024 + a * 256 + lane * 4, nosent(cvtpk(s.x, s.y)));
    }
    STAMP(5);
  } else if (wr == 8) {
    {
      const u32x4 s4 = {SENT, SENT, SENT, SENT};
      st16(ws, WS_O2 + (g * 32 + i) * 1024 + lane * 16, s4);
      if (lane == 0) st16(ws, WS_LSE + (g * 32 + i) * 128, s4);
      if (is_wo) st16(ws, WS_H3 + rg * 16384 + (g * 2 + hp) * 1024 + cb * 128 + ch * 16, s4);
      if (is_ffn) {
        st16(ws, WS_H5 + (rg * 112 + u) * 128 + ch * 16, s4);
        st16(ws, WS_H5 + ((rg + 8) * 112 + u) * 128 + ch * 16, s4);
      }
      GET_TAG();
      if (lane == 0) { st8(ws, WS_FLAG + b * 8, u32x2{tag, ~tag}); *(volatile LAS u32*)(L + L_OF + 16) = 1u; *(volatile LAS u32*)(L + L_OF + 20) = tag; }
    }
    STAMP(2);
    {
      u32x4 mm, ll; u32 spins = 0;
      do { __builtin_amdgcn_s_sleep(1); mm = *(volatile LAS u32x4*)(L + L_MM + (lane & 3) * 16); ll = *(volatile LAS u32x4*)(L + L_LL + (lane & 3) * 16); } while (__any(has_sent(ll) | has_sent(mm)) && ++spins < SPIN_CAP);
      const float M = fmaxf(fmaxf(__uint_as_float(mm.x), __uint_as_float(mm.y)), fmaxf(__uint_as_float(mm.z), __uint_as_float(mm.w)));
      const float Ls = (__uint_as_float(ll.x) + __uint_as_float(ll.y)) + (__uint_as_float(ll.z) + __uint_as_float(ll.w));
      if (lane < 4) st4(ws, WS_LSE + (g * 32 + i) * 128 + lane * 4, nosent(__float_as_uint(fmaf(M, 1.4426950408889634f, __builtin_amdgcn_logf(Ls)))));
    }
    STAMP(3);
    {
      u32x4 v0, v1; u32 spins = 0; bool bad;
      do {
        asm volatile("global_load_dwordx4 %0, %2, %3 sc1\n\tglobal_load_dwordx4 %1, %2, %3 offset:1024 sc1\n\ts_waitcnt vmcnt(0)"
                     : "=&v"(v0), "=&v"(v1) : "v"(WS_FLAG + lane * 16), "s"(ws) : "memory");
        bad = (((v0.x ^ tag) | (v0.z ^ tag)) | ((v1.x ^ tag) | (v1.z ^ tag)) | ((v0.y ^ ~tag) | (v0.w ^ ~tag)) | ((v1.y ^ ~tag) | (v1.w ^ ~tag))) != 0u;
      } while (bad && ++spins < SPIN_CAP);
    }
    STAMP(4);
    if (is_wo) {
      u32 lv, spins = 0;
      do {
        asm volatile("global_load_dword %0, %1, %2 sc1\n\ts_waitcnt vmcnt(0)" : "=v"(lv) : "v"(WS_LSE + (g * 32 + (lane & 31)) * 128 + (hp * 2 + (lane >> 5)) * 4), "s"(ws) : "memory");
      } while (lv == SENT && ++spins < SPIN_CAP);
      const float ls = __uint_as_float(lv);
      float m = fmaxf(ls, dpp<XOR1>(ls)); m = fmaxf(m, dpp<XOR2>(m)); m = fmaxf(m, dpp<ROR4>(m)); m = fmaxf(m, dpp<ROR8>(m)); m = sw16_max(m);
      const float e = __builtin_amdgcn_exp2f(ls - m);
      float s = row16_sum(e); s = sw16_add(s, s);
      *(LAS float*)(L + L_WT + lane * 4) = e * __builtin_amdgcn_rcpf(s);
    }
    STAMP(5);
  } else {
    GET_TAG();
    {
      u32x4 q4; u32 spins = 0;
      do { __builtin_amdgcn_s_sleep(1); q4 = *(volatile LAS u32x4*)(L + L_QS + lane * 16); } while (__any(has_sent(q4)) && ++spins < SPIN_CAP);
    }
    STAMP(2);
    if (is_wo) {
      const GAS char* src = (const GAS char*)wo_ + (size_t)(g * 512 + hp * 256 + rg) * 1024 + cb * 128 + ch * 16;
#pragma unroll 1
      for (int k = 0; k < 32; ++k) __builtin_amdgcn_global_load_lds((const GAS u32*)(src + k * 8192), (LAS u32*)(L + L_W + k * 1024), 16, 0, 2);
    } else if (is_ffn) {
      const size_t o = (size_t)(lane >> 1) * (FF * 2) + u * 32 + (lane & 1) * 16;
#pragma unroll 1
      for (int k = 0; k < 16; ++k) __builtin_amdgcn_global_load_lds((const GAS u32*)((const GAS char*)w1_ + o + (size_t)k * 32 * FF * 2), (LAS u32*)(L + L_W + k * 1024), 16, 0, 2);
#pragma unroll 1
      for (int k = 0; k < 16; ++k) __builtin_amdgcn_global_load_lds((const GAS u32*)((const GAS char*)w3_ + o + (size_t)k * 32 * FF * 2), (LAS u32*)(L + L_W + 16384 + k * 1024), 16, 0, 2);
#pragma unroll 1
      for (int k = 0; k < 16; ++k) __builtin_amdgcn_global_load_lds((const GAS u32*)((const GAS char*)w2_ + (size_t)(u * 16 + k) * 1024 + lane * 16), (LAS u32*)(L + L_W + 32768 + k * 1024), 16, 0, 2);
    }
    STAMP(3);
    __builtin_amdgcn_s_waitcnt(0x0F70);
    STAMP(4);
  }
  BARRIER();
  STAMP(6);
  if (w >= 8) {
  } else if (is_wo) {
    u32x4 bf[4];
#pragma unroll
    for (int ct = 0; ct < 4; ++ct) {
      LAS char* p = L + L_W + (32 * w + 8 * c + (n >> 2)) * 128 + ct * 32 + (n & 3) * 8;
      const u32x2 lo = trd(p), hi = trd(p + 512);
      bf[ct] = u32x4{lo.x, lo.y, hi.x, hi.y};
    }
    const f32x4 wt0 = *(LAS f32x4*)(L + L_WT + (w >> 2) * 128 + c * 16), wt1 = *(LAS f32x4*)(L + L_WT + (w >> 2) * 128 + 64 + c * 16);
    u32x4 a0, a1;
    const u32 o = WS_O2 + (g * 32 + n) * 1024 + hp * 512 + w * 64 + c * 16;
    gather2(ws, o, o + 16384, a0, a1);
    STAMP(7);
    float y[4];
#pragma unroll
    for (int ct = 0; ct < 4; ++ct) {
      const f32x4 d0 = mfma(a0, bf[ct], f32x4{0.f, 0.f, 0.f, 0.f}), d1 = mfma(a1, bf[ct], f32x4{0.f, 0.f, 0.f, 0.f});
      const f32x4 e = d0 * wt0 + d1 * wt1;
      y[ct] = (e[0] + e[1]) + (e[2] + e[3]);
    }
    const float z = sw16_add(sw32_add(y[0], y[2]), sw32_add(y[1], y[3]));
    *(LAS float*)(L + L_RED + w * 256 + lane * 4) = z;
    BARRIER();
    if (w == 0) {
      f32x4 s0 = {0.f, 0.f, 0.f, 0.f}, s1 = s0;
#pragma unroll
      for (int k = 0; k < 8; ++k) { s0 += *(LAS f32x4*)(L + L_RED + k * 256 + (lane & 7) * 32); s1 += *(LAS f32x4*)(L + L_RED + k * 256 + (lane & 7) * 32 + 16); }
      st16(ws, WS_H3 + (lane >> 3) * 16384 + (g * 2 + hp) * 1024 + cb * 128 + (lane & 7) * 16,
           u32x4{nosent(cvtpk(s0[0], s0[1])), nosent(cvtpk(s0[2], s0[3])), nosent(cvtpk(s1[0], s1[1])), nosent(cvtpk(s1[2], s1[3]))});
    }
    STAMP(8);
  } else if (is_ffn) {
    u32x4 b1[2], b3[2], w2a, w2b;
#pragma unroll
    for (int s = 0; s < 2; ++s) {
      LAS char* p = L + L_W + (64 * w + 32 * s + 8 * c + (n >> 2)) * 32 + (n & 3) * 8;
      u32x2 lo = trd(p), hi = trd(p + 128);
      b1[s] = u32x4{lo.x, lo.y, hi.x, hi.y};
      lo = trd(p + 16384); hi = trd(p + 16384 + 128);
      b3[s] = u32x4{lo.x, lo.y, hi.x, hi.y};
    }
    {
      LAS char* p = L + L_W + 32768 + (n >> 2) * 1024 + (w * 64 + c * 16 + (n & 3) * 4) * 2;
      const u32x2 r0 = trd(p), r1 = trd(p + 4096), r2 = trd(p + 8192), r3 = trd(p + 12288);
      w2a = u32x4{r0.x, r0.y, r1.x, r1.y}; w2b = u32x4{r2.x, r2.y, r3.x, r3.y};
    }
    const int pg = rg, col = 64 * w + 8 * ch + pg;
    const float xv = __uint_as_float((u32)(*(const GAS u16*)(x + col * 2)) << 16);
    u32x4 a0, a1;
    const u32 o = WS_H3 + g * 16384 + pg * 1024 + w * 128 + ch * 16;
    gather2(ws, o, o + 8192, a0, a1);
    STAMP(7);
    float s[8];
#pragma unroll
    for (int d = 0; d < 4; ++d) { s[2 * d] = bflo(a0[d]) + bflo(a1[d]); s[2 * d + 1] = bfhi(a0[d]) + bfhi(a1[d]); }
    const float at = treduce8(s, lane);
    const u32 hb = cvtpk(xv + rbf(at), 0.f);
    *(LAS u16*)(L + L_HB + col * 2) = (u16)hb;
    const float h = bflo(hb);
    const float s1 = wave_sum(h), s2 = wave_sum(h * h);
    LDS_FENCE();
    u32x4 h0 = *(LAS u32x4*)(L + L_HB + w * 128 + c * 16), h1 = *(LAS u32x4*)(L + L_HB + w * 128 + 64 + c * 16);
    if (n & 1) { h0 = u32x4{0x3F803F80u, 0x3F803F80u, 0x3F803F80u, 0x3F803F80u}; h1 = h0; }
    const f32x4 dg = mfma(h1, b1[1], mfma(h0, b1[0], f32x4{0.f, 0.f, 0.f, 0.f}));
    const f32x4 du = mfma(h1, b3[1], mfma(h0, b3[0], f32x4{0.f, 0.f, 0.f, 0.f}));
    if (lane < 32) *(LAS f32x2*)(L + L_RED + w * 256 + lane * 8) = c == 0 ? f32x2{dg[0], dg[1]} : f32x2{du[0], du[1]};
    if (lane == 0) *(LAS f32x2*)(L + L_ST + w * 8) = f32x2{s1, s2};
    BARRIER();
    f32x2 r = {0.f, 0.f};
#pragma unroll
    for (int k = 0; k < 4; ++k) r += *(LAS f32x2*)(L + L_RED + ((lane >> 5) * 4 + k) * 256 + (lane & 31) * 8);
    f32x2 st = *(LAS f32x2*)(L + L_ST + (lane & 7) * 8);
    st.x += dpp<XOR1>(st.x); st.y += dpp<XOR1>(st.y); st.x += dpp<XOR2>(st.x); st.y += dpp<XOR2>(st.y); st.x += dpp<XOR7>(st.x); st.y += dpp<XOR7>(st.y);
    const float mean = st.x * (1.0f / 512), var = fmaxf(fmaf(-mean, mean, st.y * (1.0f / 512)), 0.f);
    const float mb = rbf(mean), rden = __builtin_amdgcn_rcpf(rbf(__builtin_amdgcn_sqrtf(rbf(rbf(var) + 1e-5f))));
    const float ac = sw32_add(r.x, r.x), cs = sw32_add(r.y, r.y);
    const float v = rbf(fmaf(-mb, cs, ac) * rden);
    auto gu = __builtin_amdgcn_permlane16_swap(__float_as_uint(v), __float_as_uint(v), false, false);
    const float gt = __uint_as_float(gu[0]), up = __uint_as_float(gu[1]);
    const float sl = rbf(gt * __builtin_amdgcn_rcpf(1.0f + __builtin_amdgcn_exp2f(gt * -1.4426950408889634f)));
    const float act = sl * up;
    const u32 ap = cvtpk(act, dpp<XOR1>(act));
    float y = 0.f;
#pragma unroll
    for (int k = 0; k < 4; ++k) y = dot2(w2a[k], __builtin_amdgcn_readlane(ap, 2 * k), y);
    float y2 = 0.f;
#pragma unroll
    for (int k = 0; k < 4; ++k) y2 = dot2(w2b[k], __builtin_amdgcn_readlane(ap, 8 + 2 * k), y2);
    st4(ws, WS_H5 + ((2 * w + (lane >> 5)) * 112 + u) * 128 + (lane & 31) * 4, nosent(__float_as_uint(y + y2)));
    STAMP(8);
  } else {
    float hv = 0.f;
    if (w == 7) {
      const int q4 = lane & 3, p = lane >> 2;
      u32x4 a0; u32 spins = 0;
      do {
        asm volatile("global_load_dwordx4 %0, %1, %2 sc1\n\ts_waitcnt vmcnt(0)" : "=v"(a0) : "v"(WS_H3 + g * 16384 + p * 1024 + rc * 64 + q4 * 16), "s"(ws) : "memory");
      } while (has_sent(a0) && ++spins < SPIN_CAP);
      float s[8];
#pragma unroll
      for (int d = 0; d < 4; ++d) { s[2 * d] = bflo(a0[d]); s[2 * d + 1] = bfhi(a0[d]); }
      float t[4], e[2];
#pragma unroll
      for (int k = 0; k < 4; ++k) t[k] = sw32_add(s[k], s[k + 4]);
      e[0] = sw16_add(t[0], t[2]); e[1] = sw16_add(t[1], t[3]);
      const float f0 = e[0] + dpp<ROR8>(e[0]), f1 = e[1] + dpp<ROR8>(e[1]);
      float at = (lane & 8) ? f1 : f0;
      at += __shfl_xor(at, 4);
      const int col = 8 * q4 + (lane >> 3);
      const float xv = __uint_as_float((u32)(*(const GAS u16*)(x + (32 * rc + col) * 2)) << 16);
      *(LAS float*)(L + L_RED + 7 * 128 + col * 4) = rbf(xv + rbf(at));
      STAMP(7);
    } else {
      u32x4 a0, a1;
      const u32 o = WS_H5 + rc * (112 * 128) + w * 2048 + lane * 16;
      gather2(ws, o, o + 1024, a0, a1);
      STAMP(7);
      const f32x4 s = __builtin_bit_cast(f32x4, a0) + __builtin_bit_cast(f32x4, a1);
      float z = sw16_add(sw32_add(s[0], s[2]), sw32_add(s[1], s[3]));
      z += dpp<ROR8>(z);
      *(LAS float*)(L + L_RED + w * 128 + ((lane & 7) * 4 + c) * 4) = z;
    }
    BARRIER();
    if (tid < 16) {
      f32x2 s = {0.f, 0.f};
#pragma unroll
      for (int k = 0; k < 7; ++k) s += *(LAS f32x2*)(L + L_RED + k * 128 + tid * 8);
      const f32x2 h = *(LAS f32x2*)(L + L_RED + 7 * 128 + tid * 8);
      *(GAS u32*)((GAS char*)out_ + rc * 64 + tid * 4) = cvtpk(h.x + rbf(s.x), h.y + rbf(s.y));
      if (rc == 0 && tid == 0) st4(ws, WS_EPOCH, tag);
    }
    (void)hv;
    STAMP(8);
  }
  if (w == 0 && (true)) {
    const u64 tgt = ts[0] + 4000ull + 0ull * (ts[0] - ts[0]);
    u64 now; u32 sp = 0;
    do { asm volatile("s_memrealtime %0\n\ts_waitcnt lgkmcnt(0)" : "=s"(now) :: "memory"); __builtin_amdgcn_s_sleep(2); } while (now < tgt && ++sp < 100000u);
  }
}

extern "C" void kernel_launch(void* const* d_in, const int* in_sizes, int n_in,
                              void* d_out, int out_size, void* d_ws, size_t ws_size,
                              hipStream_t stream) {
  fused_decode<<<256, NTHR, 0, stream>>>((const char*)d_in[0], (const char*)d_in[1], (const char*)d_in[2], (const char*)d_in[3],
                                        (const char*)d_in[4], (const char*)d_in[6], (const char*)d_in[7], (const char*)d_in[8],
                                        (char*)d_out, (char*)d_ws);
}
```

```cpp
#include <hip/hip_runtime.h>
#include <hip/hip_bf16.h>
#include <stdint.h>

typedef unsigned short u16;
typedef unsigned int u32;
typedef unsigned long long u64;
typedef u32 u32x4 __attribute__((ext_vector_type(4)));
typedef u32 u32x2 __attribute__((ext_vector_type(2)));
typedef u64 u64x8 __attribute__((ext_vector_type(8)));
typedef u64 u64x2 __attribute__((ext_vector_type(2)));
typedef float f32x4 __attribute__((ext_vector_type(4)));
typedef float f32x2 __attribute__((ext_vector_type(2)));
typedef short s16x8 __attribute__((ext_vector_type(8)));
typedef short s16x4 __attribute__((ext_vector_type(4)));
typedef __bf16 bf16x2 __attribute__((ext_vector_type(2)));
#define GAS __attribute__((address_space(1)))
#define LAS __attribute__((address_space(3)))

constexpr int SK = 65536, WIN = 4096, FF = 1792;
constexpr u32 WS_EPOCH = 0;
constexpr u32 WS_CF = 12288;
constexpr u32 WS_QG = 28672;
constexpr u32 WS_LSE = WS_QG + 8 * 8192;
constexpr u32 WS_O2 = WS_LSE + 128 * 128;
constexpr u32 WS_H3 = WS_O2 + 128 * 1024;
constexpr u32 WS_H5 = WS_H3 + 8 * 16384;
constexpr u32 L_K = 0;
constexpr u32 L_V = 65536;
constexpr u32 L_WO = 0;
constexpr u32 L_W1 = 32768;
constexpr u32 L_W2 = 65536;
constexpr u32 L_INIT = 131072;
constexpr u32 L_QP = L_INIT;
constexpr u32 L_MM = L_QP + 2048;
constexpr u32 L_FQ = L_MM + 128;
constexpr u32 L_KR = L_FQ + 16;
constexpr u32 L_FO = L_FQ + 32 + 16;
constexpr u32 L_FP = L_FQ + 32;
constexpr u32 L_FK = L_FP + 32;
constexpr u32 L_PV = L_FK + 8;
constexpr u32 L_TG = L_PV + 8;
constexpr u32 L_FB = L_TG + 8 + 8;
constexpr u32 L_FG = L_FB + 16;
constexpr u32 L_QS = L_INIT + 2816;
constexpr u32 L_PF = L_QS + 1024;
constexpr u32 L_LL = L_PF + 8192;
constexpr u32 L_WT = L_LL + 128;
constexpr u32 L_RED = L_WT + 128;
constexpr u32 L_TOTAL = L_RED + 2048 + 4096;
static_assert(L_TOTAL <= 160 * 1024 && L_TOTAL > 80 * 1024 && L_FB % 16 == 0 && L_FQ % 16 == 0, "lds");
constexpr u32 SENT = 0xFFFFFFFFu;
constexpr u32 SPIN_CAP = 1u << 18;
constexpr u32 LDS_CAP = 1u << 22;

__device__ __forceinline__ float bflo(u32 w) { return __uint_as_float(w << 16); }
__device__ __forceinline__ float bfhi(u32 w) { return __uint_as_float(w & 0xffff0000u); }
__device__ __forceinline__ u32 cvtpk(float lo, float hi) { return __builtin_bit_cast(u32, __builtin_convertvector(f32x2{lo, hi}, bf16x2)); }
__device__ __forceinline__ float rbf(float f) { return bflo(cvtpk(f, f)); }
__device__ __forceinline__ u32 nosent(u32 w) { return w < 0xFFFFFFFEu ? w : 0xFFFFFFFEu; }
__device__ __forceinline__ float dot2(u32 a, u32 b, float c) { return __builtin_amdgcn_fdot2_f32_bf16(__builtin_bit_cast(bf16x2, a), __builtin_bit_cast(bf16x2, b), c, false); }
__device__ __forceinline__ f32x4 mfma(u32x4 a, u32x4 b, f32x4 c) { return __builtin_amdgcn_mfma_f32_16x16x32_bf16(__builtin_bit_cast(s16x8, a), __builtin_bit_cast(s16x8, b), c, 0, 0, 0); }
template <int CTRL> __device__ __forceinline__ float dpp(float x) { return __builtin_bit_cast(float, __builtin_amdgcn_mov_dpp(__builtin_bit_cast(int, x), CTRL, 0xf, 0xf, true)); }
constexpr int XOR1 = 0xB1, XOR2 = 0x4E, XOR7 = 0x141, ROR8 = 0x128, ROR4 = 0x124;
__device__ __forceinline__ float sw32_add(float a, float b) { auto r = __builtin_amdgcn_permlane32_swap(__float_as_uint(a), __float_as_uint(b), false, false); return __uint_as_float(r[0]) + __uint_as_float(r[1]); }
__device__ __forceinline__ float sw16_add(float a, float b) { auto r = __builtin_amdgcn_permlane16_swap(__float_as_uint(a), __float_as_uint(b), false, false); return __uint_as_float(r[0]) + __uint_as_float(r[1]); }
__device__ __forceinline__ float sw32_max(float a) { auto r = __builtin_amdgcn_permlane32_swap(__float_as_uint(a), __float_as_uint(a), false, false); return fmaxf(__uint_as_float(r[0]), __uint_as_float(r[1])); }
__device__ __forceinline__ float sw16_max(float a) { auto r = __builtin_amdgcn_permlane16_swap(__float_as_uint(a), __float_as_uint(a), false, false); return fmaxf(__uint_as_float(r[0]), __uint_as_float(r[1])); }
__device__ __forceinline__ float row16_sum(float v) { v += dpp<XOR1>(v); v += dpp<XOR2>(v); v += dpp<ROR4>(v); v += dpp<ROR8>(v); return v; }
__device__ __forceinline__ float wave_sum(float v) { v = row16_sum(v); v = sw16_add(v, v); return sw32_add(v, v); }
__device__ __forceinline__ float treduce8(const float (&a)[8], int lane) {
  float t0 = sw32_add(a[0], a[4]), t1 = sw32_add(a[1], a[5]), t2 = sw32_add(a[2], a[6]), t3 = sw32_add(a[3], a[7]);
  float u0 = sw16_add(t0, t2), u1 = sw16_add(t1, t3);
  float v0 = u0 + dpp<ROR8>(u0), v1 = u1 + dpp<ROR8>(u1);
  return (lane & 8) ? v1 : v0;
}
__device__ __forceinline__ u32 permlo(u32 hi, u32 lo) { return __builtin_amdgcn_perm(hi, lo, 0x05040100u); }
__device__ __forceinline__ u32 permhi(u32 hi, u32 lo) { return __builtin_amdgcn_perm(hi, lo, 0x07060302u); }
__device__ __forceinline__ u32x2 trd(LAS char* p) { return __builtin_bit_cast(u32x2, __builtin_amdgcn_ds_read_tr16_b64_v4i16((LAS s16x4*)p)); }
__device__ __forceinline__ void st4(GAS char* b, u32 off, u32 v) { asm volatile("global_store_dword %0, %1, %2 sc1" :: "v"(off), "v"(v), "s"(b) : "memory"); }
__device__ __forceinline__ void st8(GAS char* b, u32 off, u32x2 v) { asm volatile("global_store_dwordx2 %0, %1, %2 sc1" :: "v"(off), "v"(v), "s"(b) : "memory"); }
__device__ __forceinline__ bool has_sent(u32x4 v) { return max(max(v.x, v.y), max(v.z, v.w)) == SENT; }
#define BARRIER() do { asm volatile("s_waitcnt lgkmcnt(0)" ::: "memory"); __builtin_amdgcn_s_barrier(); asm volatile("" ::: "memory"); } while (0)

__device__ __forceinline__ void gather2(const GAS char* b, u32 o0, u32 o1, u32x4& v0, u32x4& v1) {
  u32 spins = 0; bool bad;
  do {
    asm volatile("global_load_dwordx4 %0, %2, %4 sc1\n\tglobal_load_dwordx4 %1, %3, %4 sc1\n\ts_waitcnt vmcnt(0)"
                 : "=&v"(v0), "=&v"(v1) : "v"(o0), "v"(o1), "s"(b) : "memory");
    bad = has_sent(v0) | has_sent(v1);
  } while (bad && ++spins < SPIN_CAP);
}

__device__ __forceinline__ float sum4(f32x4 d) { const f32x2 e = f32x2{d[0], d[1]} + f32x2{d[2], d[3]}; return e[0] + e[1]; }

#define LD32(o) (*(volatile LAS u32*)(L + (o)))
#define LD64(o) (*(volatile LAS u32x2*)(L + (o)))
#define LD128(o) (*(volatile LAS u32x4*)(L + (o)))
#define SETF(o) do { if (lane == 0) LD32(o) = 1u; } while (0)
#define WAITF(o, slp) do { u32 sp_ = 0; while (__builtin_amdgcn_readfirstlane(LD32(o)) == SENT && ++sp_ < LDS_CAP) { if (slp) __builtin_amdgcn_s_sleep(1); } asm volatile("" ::: "memory"); } while (0)
#define READ_TAG() do { u32x2 tg_; u32 sp_ = 0; do { tg_ = LD64(L_TG); } while (__builtin_amdgcn_readfirstlane(tg_.x) == SENT && ++sp_ < LDS_CAP); tag = __builtin_amdgcn_readfirstlane(tg_.y); } while (0)
#define GLD(win, pol, base, off, lds) asm volatile("s_mov_b32 m0, %2\n\ts_waitcnt vmcnt(" win ")\n\tglobal_load_lds_dwordx4 %0, %1" pol :: "v"(off), "s"(base), "s"(lds) : "memory")
#define NOW_CLK() asm volatile("s_memtime %0\n\ts_waitcnt lgkmcnt(0)" : "=s"(clk0) :: "memory")
#define WAIT_CLK(cyc) do { u64 now_; do { asm volatile("s_sleep 1\n\ts_memtime %0\n\ts_waitcnt lgkmcnt(0)" : "=s"(now_) :: "memory"); } while (now_ - clk0 < (u64)(cyc)); } while (0)
constexpr int R_Q = 3300, R_CA = 2500, R_C = 6000, R_L = 13000, R_W = 4000, O2DELAY = 12, LDL = 8, H5DELAY = 20;
#define VD "15"
#define KFI "4"
constexpr int NTHR = 704;
constexpr u32 CODE_BYTES = 11392;
__global__ void __launch_bounds__(NTHR, 3)
fused_decode(const char* a0_, const char* a1_, const char* a2_, const char* a3_, const char* a4_, const char* a5_, const char* a6_, const char* a7_,
             char* a8_, char* a9_) {
  __shared__ __attribute__((aligned(16))) char smem[L_TOTAL];
  LAS char* L = (LAS char*)smem;
  u64x8 ka; u64x2 kb; u64 clk0;
  asm volatile("s_load_dwordx16 %0, %2, 0x0\n\ts_load_dwordx4 %1, %2, 0x40" : "=&s"(ka), "=&s"(kb) : "s"(__builtin_amdgcn_kernarg_segment_ptr()) : "memory");
  const int tid = threadIdx.x, lane = tid & 63, w = __builtin_amdgcn_readfirstlane(tid >> 6), t = w - 3;
  u32 codepf;
  {
    u64 pc;
    asm volatile("s_getpc_b64 %0" : "=s"(pc));
    asm volatile("global_load_dword %0, %1, %2" : "=v"(codepf) : "v"(((blockIdx.x & 8) || tid < 576) ? 0u : min((u32)(tid - 576) * 128u, CODE_BYTES - 4u)), "s"(pc & ~255ull) : "memory");
  }
  const int b = blockIdx.x, g = b & 7, j = b >> 4, tail = (b >> 3) & 1;
  const int n = lane & 15, c = lane >> 4, ch = lane & 7, rg = lane >> 3;
  const int hp = j >> 3, cb = j & 7;
  const int noffn = (g >> 2) & (j >> 2) & (j >> 3) & 1;
  const bool is_ffn = noffn == 0;
  const int u = (g < 4 ? 16 * g : 16 + 12 * g) + j;

  LD32(L_INIT + tid * 4) = SENT;
  const u32 row0 = 256 * hp + 32 * t + 4 * rg;
  u32 oq = row0 * 8192 + (g * 512 + cb * 64) * 2 + ch * 16, ox = lane * 16, oxr = (row0 & ~7u) * 2, zero = 0;
  u32 ok0 = w * 4096 + c * 256 + ((n ^ c) * 16), ok1 = ok0 ^ 64, ok2 = ok0 ^ 128, ok3 = ok0 ^ 192;
  u32 ov0 = w * 4096 + c * 256 + ((n ^ (c << 1)) * 16), ov1 = ov0 ^ 128, fl0 = L_FK + w * 4, cnt = 1;
  u64 kvo = ((u64)(g * SK + (SK - WIN) + j * 256)) * 256;
  u32 oc0 = WS_O2 + (g * 16 + j) * 1024 + lane * 16, oc1 = WS_H3 + rg * 16384 + (g * 2 + hp) * 1024 + cb * 128 + ch * 16;
  u32 oc2 = WS_H5 + (rg * 112 + min(u, 111)) * 128 + ch * 16, ocf = WS_CF + (g * 16 + j) * 8 + lane * 4, cfm = 0u - (u32)lane;
  u32x4 s4 = {SENT, SENT, SENT, SENT};
  int mode = tail * 16 + w;
  asm volatile("" : "+v"(oq), "+v"(ox), "+v"(oxr), "+v"(zero), "+v"(oc0), "+v"(oc1), "+v"(oc2), "+v"(ocf), "+v"(cfm), "+v"(s4),
                    "+v"(ok0), "+v"(ok1), "+v"(ok2), "+v"(ok3), "+v"(ov0), "+v"(ov1), "+v"(cnt), "+v"(fl0), "+s"(mode));
  asm volatile("s_waitcnt lgkmcnt(0)" : "+s"(ka), "+s"(kb), "+s"(kvo) :: "memory");
  const u64 x_ = ka[0], kc_ = ka[1], vc_ = ka[2], wq_ = ka[3], wo_ = ka[4], w1_ = ka[5], w3_ = ka[6], w2_ = ka[7], out_ = kb[0], ws_ = kb[1];
  const GAS char* x = (const GAS char*)x_;
  GAS char* ws = (GAS char*)ws_;
  __builtin_amdgcn_s_barrier();

  u32x4 q_[6];
  u32 tag;
  asm volatile(
      "s_cmp_lt_u32 %[m], 19\n\t"
      "s_cbranch_scc1 1f\n\t"
      "global_load_dwordx4 %[q4], %[ox], %[x]\n\t"
      "global_load_dwordx4 %[q5], %[oxr], %[x]\n\t"
      "global_load_dwordx4 %[q0], %[oq], %[wq] nt\n\t"
      "global_load_dwordx4 %[q1], %[oq], %[wq1] nt\n\t"
      "global_load_dwordx4 %[q2], %[oq], %[wq2] nt\n\t"
      "global_load_dwordx4 %[q3], %[oq], %[wq3] nt\n\t"
      "s_branch 2f\n"
      "1:\n\t"
      "s_cmp_lg_u32 %[w], 2\n\t"
      "s_cbranch_scc1 2f\n\t"
      "global_load_dword %[z], %[z], %[ws] sc1\n\t"
      "s_cmp_lg_u32 %[m], 2\n\t"
      "s_cbranch_scc1 3f\n\t"
      "global_store_dwordx4 %[c0], %[s4], %[ws] sc1\n\t"
      "global_store_dwordx4 %[c1], %[s4], %[ws] sc1\n\t"
      "s_cmp_lg_u32 %[nf], 0\n\t"
      "s_cbranch_scc1 3f\n\t"
      "global_store_dwordx4 %[c2], %[s4], %[ws] sc1\n\t"
      "global_store_dwordx4 %[c2], %[s4], %[ws5] sc1\n"
      "3:\n\t"
      "s_waitcnt vmcnt(0)\n\t"
      "v_readfirstlane_b32 %[tag], %[z]\n\t"
      "s_nop 0\n\t"
      "s_add_u32 %[tag], %[tag], 1\n\t"
      "v_mov_b32 %[z], %[tag]\n\t"
      "s_mov_b64 exec, 1\n\t"
      "ds_write_b32 %[fl], %[z] offset:12\n\tds_write_b32 %[fl], %[one] offset:8\n\t"
      "s_cmp_lg_u32 %[m], 2\n\t"
      "s_cbranch_scc1 6f\n\t"
      "s_mov_b64 exec, 3\n\t"
      "v_xor_b32 %[z], %[tag], %[cm]\n\t"
      "global_store_dword %[cf], %[z], %[ws] sc1\n"
      "6:\n\t"
      "s_mov_b64 exec, -1\n"
      "2:"
      : [q0] "=&v"(q_[0]), [q1] "=&v"(q_[1]), [q2] "=&v"(q_[2]), [q3] "=&v"(q_[3]), [q4] "=&v"(q_[4]), [q5] "=&v"(q_[5]), [tag] "=&s"(tag), [z] "+v"(zero)
      : [w] "s"(w), [m] "s"(mode), [ox] "v"(ox), [oxr] "v"(oxr), [oq] "v"(oq), [x] "s"(x_), [wq] "s"(wq_), [wq1] "s"(wq_ + 8192), [wq2] "s"(wq_ + 16384),
        [wq3] "s"(wq_ + 24576), [c0] "v"(oc0), [c1] "v"(oc1), [c2] "v"(oc2), [cf] "v"(ocf), [cm] "v"(cfm), [nf] "s"(noffn), [s4] "v"(s4), [ws] "s"(ws_), [ws5] "s"(ws_ + 8 * 112 * 128),
        [fl] "v"(fl0), [one] "v"(cnt)
      : "memory", "scc");
  {
    u32 sl, se;
    asm volatile(
        "s_cmp_gt_u32 %[m], 1\n\t"
        "s_cbranch_scc1 9f\n\t"
        "s_sleep 36\n\t"
        "s_lshl_b32 %[sl], %[m], 12\n\t"
        "s_add_u32 %[se], %[sl], 0x10000+" KFI "*0x2000\n"
        "1:\n\t"
        "s_mov_b32 m0, %[sl]\n\t"
        "s_waitcnt vmcnt(" VD ")\n\tglobal_load_lds_dwordx4 %[k0], %[kb] nt\n\t"
        "s_waitcnt vmcnt(" VD ")\n\tglobal_load_lds_dwordx4 %[k1], %[kb] offset:1024 nt\n\t"
        "s_waitcnt vmcnt(" VD ")\n\tglobal_load_lds_dwordx4 %[k2], %[kb] offset:2048 nt\n\t"
        "s_waitcnt vmcnt(" VD ")\n\tglobal_load_lds_dwordx4 %[k3], %[kb] offset:3072 nt\n\t"
        "v_add_u32 %[k0], 0x2000, %[k0]\n\tv_add_u32 %[k1], 0x2000, %[k1]\n\tv_add_u32 %[k2], 0x2000, %[k2]\n\tv_add_u32 %[k3], 0x2000, %[k3]\n\t"
        "s_add_u32 %[sl], %[sl], 0x2000\n\t"
        "s_cmp_lt_u32 %[sl], 0x10000\n\t"
        "s_cbranch_scc1 1b\n"
        "2:\n\t"
        "s_mov_b32 m0, %[sl]\n\t"
        "s_waitcnt vmcnt(" VD ")\n\tglobal_load_lds_dwordx4 %[v0], %[vb] nt\n\t"
        "s_waitcnt vmcnt(" VD ")\n\tglobal_load_lds_dwordx4 %[v1], %[vb] offset:1024 nt\n\t"
        "s_waitcnt vmcnt(" VD ")\n\tglobal_load_lds_dwordx4 %[v0], %[vb] offset:2048 nt\n\t"
        "s_waitcnt vmcnt(" VD ")\n\tglobal_load_lds_dwordx4 %[v1], %[vb] offset:3072 nt\n\t"
        "v_add_u32 %[v0], 0x2000, %[v0]\n\tv_add_u32 %[v1], 0x2000, %[v1]\n\t"
        "s_add_u32 %[sl], %[sl], 0x2000\n\t"
        "s_cmp_lg_u32 %[sl], %[se]\n\t"
        "s_cbranch_scc1 3f\n\t"
        "s_mov_b64 exec, 1\n\tds_write_b32 %[fl], %[cnt]\n\ts_mov_b64 exec, -1\n"
        "3:\n\t"
        "s_cmp_lt_u32 %[sl], 0x20000\n\t"
        "s_cbranch_scc1 2b\n"
        "9:"
        : [sl] "=&s"(sl), [se] "=&s"(se), [k0] "+v"(ok0), [k1] "+v"(ok1), [k2] "+v"(ok2), [k3] "+v"(ok3), [v0] "+v"(ov0), [v1] "+v"(ov1)
        : [m] "s"(mode), [kb] "s"(kc_ + kvo), [vb] "s"(vc_ + kvo), [fl] "v"(fl0), [cnt] "v"(cnt), "v"(codepf)
        : "memory", "scc");
  }

  if (!tail) {
    if (w < 2) {
      { u32 sp = 0; while (__any(LD32(L_KR + (lane & 3) * 4) == SENT) && ++sp < LDS_CAP) {} asm volatile("" ::: "memory"); }
      u32 o = (u32)(g * 512 + hp * 256 + 8 * w + rg) * 1024 + cb * 128 + ch * 16;
#pragma unroll
      for (u32 k = 0; k < 16; ++k) {
        GLD(VD, " nt", wo_, o, L_WO + (2 * k + w) * 1024);
        o += 16384;
        if (k == 7 && lane == 0) LD32(L_PV + w * 4) = 6u;
      }
      asm volatile("s_waitcnt vmcnt(16)" ::: "memory");
      if (lane == 0) LD32(L_PV + w * 4) = 8u;
      asm volatile("s_waitcnt vmcnt(0)" ::: "memory");
      SETF(L_FB + w * 4);
    } else if (w >= 3) {
    if (w >= 7) {
      const int a = t - 4;
      asm volatile("s_waitcnt vmcnt(0)" :: "v"(codepf) : "memory");
      { u32 sp = 0; while (__any(LD32(L_FK + (lane & 1) * 4) == SENT) && ++sp < LDS_CAP) __builtin_amdgcn_s_sleep(1); asm volatile("" ::: "memory"); }
      u32x4 kf[16], qf[4];
#pragma unroll
      for (int k = 0; k < 16; ++k) kf[k] = *(LAS u32x4*)(L + L_K + (64 * a + 16 * (k >> 2) + n) * 256 + (((4 * (k & 3) + c) ^ n) * 16));
      asm volatile("s_waitcnt lgkmcnt(0)" : "+v"(kf[0]), "+v"(kf[1]), "+v"(kf[2]), "+v"(kf[3]), "+v"(kf[4]), "+v"(kf[5]), "+v"(kf[6]), "+v"(kf[7]),
                   "+v"(kf[8]), "+v"(kf[9]), "+v"(kf[10]), "+v"(kf[11]), "+v"(kf[12]), "+v"(kf[13]), "+v"(kf[14]), "+v"(kf[15]) :: "memory");
      SETF(L_KR + a * 4);
      { u32 sp = 0; while (__any(LD32(L_FQ + (lane & 3) * 4) == SENT) && ++sp < LDS_CAP) {} asm volatile("" ::: "memory"); }
#pragma unroll
      for (int s = 0; s < 4; ++s) qf[s] = *(LAS u32x4*)(L + L_QS + (n >> 2) * 256 + s * 64 + c * 16);
      f32x4 sc[4];
#pragma unroll
      for (int T = 0; T < 4; ++T) sc[T] = f32x4{0.f, 0.f, 0.f, 0.f};
#pragma unroll
      for (int s = 0; s < 4; ++s)
#pragma unroll
        for (int T = 0; T < 4; ++T) sc[T] = mfma(kf[4 * T + s], qf[s], sc[T]);
      const f32x4 sa = (n & 1) ? sc[1] : sc[0], sb = (n & 1) ? sc[3] : sc[2], st = (n & 2) ? sb : sa;
      const u32 r0 = cvtpk(st[0], st[1]), r1 = cvtpk(st[2], st[3]);
      const u32 r2 = cvtpk(bflo(r0) * 0.08838834764831845f, bfhi(r0) * 0.08838834764831845f), r3 = cvtpk(bflo(r1) * 0.08838834764831845f, bfhi(r1) * 0.08838834764831845f);
      const float s0 = bflo(r2), s1 = bfhi(r2), s2 = bflo(r3), s3 = bfhi(r3);
      float m = fmaxf(fmaxf(s0, s1), fmaxf(s2, s3)); m = fmaxf(m, dpp<XOR1>(m)); m = fmaxf(m, dpp<XOR2>(m));
      m = sw32_max(sw16_max(m));
      if ((lane & 51) == 0) LD32(L_MM + (n >> 2) * 16 + a * 4) = nosent(__float_as_uint(m));
      u32x4 mm;
      { u32 spins = 0; do { mm = LD128(L_MM + (n >> 2) * 16); } while (__any(has_sent(mm)) && ++spins < LDS_CAP); }
      const float M = fmaxf(fmaxf(__uint_as_float(mm.x), __uint_as_float(mm.y)), fmaxf(__uint_as_float(mm.z), __uint_as_float(mm.w)));
      const float p0 = __builtin_amdgcn_exp2f((s0 - M) * 1.4426950408889634f), p1 = __builtin_amdgcn_exp2f((s1 - M) * 1.4426950408889634f);
      const float p2 = __builtin_amdgcn_exp2f((s2 - M) * 1.4426950408889634f), p3 = __builtin_amdgcn_exp2f((s3 - M) * 1.4426950408889634f);
      LD64(L_PF + (2 * a + ((n >> 1) & 1)) * 256 + (n >> 2) * 64 + c * 16 + (n & 1) * 8) = u32x2{cvtpk(p0, p1), cvtpk(p2, p3)};
      float l = (p0 + p1) + (p2 + p3); l += dpp<XOR1>(l); l += dpp<XOR2>(l);
      l = sw16_add(l, l); l = sw32_add(l, l);
      if ((lane & 51) == 0) LD32(L_LL + (n >> 2) * 16 + a * 4) = __float_as_uint(l);
      SETF(L_FP + a * 4);
      { u32 sp = 0; while (__any(LD32(L_FO + (lane & 3) * 4) == SENT) && ++sp < LDS_CAP) __builtin_amdgcn_s_sleep(1); asm volatile("" ::: "memory"); }
    } else {
      {
        u32 d0;
        asm volatile("global_load_dword %0, %1, %2\n\ts_waitcnt vmcnt(0)" : "=&v"(d0) : "v"((u32)((lane & 1) * 256 + 64 * t + j) * 8192 + g * 1024), "s"(wq_), "v"(codepf) : "memory");
        NOW_CLK();
      }
      READ_TAG();
      WAIT_CLK(R_Q);
      {
        const u32 o = WS_QG + g * 8192 + t * 1024 + lane * 16 + 2048;
        u32x4 v0, v1; u32 spins = 0;
        do {
          asm volatile("global_load_dwordx4 %0, %2, %3 offset:-2048 sc1\n\tglobal_load_dwordx4 %1, %2, %3 offset:2048 sc1\n\ts_waitcnt vmcnt(0)" : "=&v"(v0), "=&v"(v1) : "v"(o), "s"(ws) : "memory");
        } while ((((v0.y ^ tag) | (v0.w ^ tag)) | ((v1.y ^ tag) | (v1.w ^ tag))) != 0u && ++spins < SPIN_CAP);
        LD32(L_QS + t * 256 + lane * 4) = cvtpk(__uint_as_float(v0.x) + __uint_as_float(v1.x), __uint_as_float(v0.z) + __uint_as_float(v1.z));
        SETF(L_FQ + t * 4);
      }
      const u32 vrow = 4 * c + (n >> 2);
      const u32 va0 = L_V + vrow * 256 + (((4 * t + ((n >> 1) & 1)) ^ ((vrow & 7) << 1)) * 16) + (n & 1) * 8, va1 = va0 ^ 32;
      const u32 oo = WS_O2 + (g * 16 + j) * 1024 + t * 256 + (n >> 2) * 64 + (n & 1) * 32 + c * 8, pva = L_PV + (lane & 1) * 4;
      { u32 sp = 0; while (__any(LD32(L_FP + (lane & 3) * 4) == SENT) && ++sp < LDS_CAP) __builtin_amdgcn_s_sleep(1); asm volatile("" ::: "memory"); }
      u32x4 pf[8];
#pragma unroll
      for (int k = 0; k < 8; ++k) pf[k] = *(LAS u32x4*)(L + L_PF + k * 256 + (n >> 2) * 64 + c * 16);
      const f32x4 ll = *(LAS f32x4*)(L + L_LL + (n >> 2) * 16);
      { u32 sp = 0; while (__any((int)LD32(pva) < 6) && ++sp < LDS_CAP) {} asm volatile("" ::: "memory"); }
      f32x4 o0 = {0.f, 0.f, 0.f, 0.f}, o1 = o0;
      {
        u32x4 v0f[6], v1f[6];
#pragma unroll
        for (int blk = 0; blk < 6; ++blk) {
          const u32x2 a0l = trd(L + va0 + blk * 8192), a0h = trd(L + va0 + blk * 8192 + 4096), a1l = trd(L + va1 + blk * 8192), a1h = trd(L + va1 + blk * 8192 + 4096);
          v0f[blk] = u32x4{a0l.x, a0l.y, a0h.x, a0h.y}; v1f[blk] = u32x4{a1l.x, a1l.y, a1h.x, a1h.y};
        }
        __builtin_amdgcn_sched_barrier(0);
#pragma unroll
        for (int blk = 0; blk < 6; ++blk) { o0 = mfma(v0f[blk], pf[blk], o0); o1 = mfma(v1f[blk], pf[blk], o1); }
      }
      const float rL = __builtin_amdgcn_rcpf((ll[0] + ll[1]) + (ll[2] + ll[3]));
      {
        int f; u32 spins = 0; u32x2 a0l, a0h, a1l, a1h, b0l, b0h, b1l, b1h;
        do {
          asm volatile("ds_read_b32 %0, %9\n\tds_read_b64_tr_b16 %1, %10 offset:49152\n\tds_read_b64_tr_b16 %2, %10 offset:53248\n\t"
                       "ds_read_b64_tr_b16 %3, %11 offset:49152\n\tds_read_b64_tr_b16 %4, %11 offset:53248\n\t"
                       "ds_read_b64_tr_b16 %5, %10 offset:57344\n\tds_read_b64_tr_b16 %6, %10 offset:61440\n\t"
                       "ds_read_b64_tr_b16 %7, %11 offset:57344\n\tds_read_b64_tr_b16 %8, %11 offset:61440\n\ts_waitcnt lgkmcnt(0)"
                       : "=&v"(f), "=&v"(a0l), "=&v"(a0h), "=&v"(a1l), "=&v"(a1h), "=&v"(b0l), "=&v"(b0h), "=&v"(b1l), "=&v"(b1h) : "v"(pva), "v"(va0), "v"(va1), "v"(oo), "v"(rL) : "memory");
        } while (__any(f < 8) && ++spins < LDS_CAP);
        o0 = mfma(u32x4{a0l.x, a0l.y, a0h.x, a0h.y}, pf[6], o0);
        o1 = mfma(u32x4{a1l.x, a1l.y, a1h.x, a1h.y}, pf[6], o1);
        o0 = mfma(u32x4{b0l.x, b0l.y, b0h.x, b0h.y}, pf[7], o0);
        o1 = mfma(u32x4{b1l.x, b1l.y, b1h.x, b1h.y}, pf[7], o1);
      }
      const f32x4 os = ((n & 1) ? o1 : o0) * rL;
      if ((n & 2) == 0) st8(ws, oo, u32x2{nosent(cvtpk(os[0], os[1])), nosent(cvtpk(os[2], os[3]))});
      SETF(L_FO + t * 4);
    }
    {
      const u32 xw = (g | hp) == 0 ? *(const GAS u32*)(x + cb * 128 + (lane & 31) * 4) : 0u;
      const u32 o = WS_O2 + (g * 16 + n) * 1024 + (t & 3) * 256 + hp * 128 + (t >> 2) * 64 + c * 16;
      const u32 oh = WS_H3 + (2 * t + (lane >> 5)) * 16384 + (g * 2 + hp) * 1024 + cb * 128 + (lane & 31) * 4;
      u32x4 a0;
      WAITF(L_FG, 1);
      __builtin_amdgcn_s_sleep(O2DELAY);
      asm volatile("global_load_dwordx4 %0, %1, %2 sc1" : "=&v"(a0) : "v"(o), "s"(ws) : "memory");
      { u32 sp = 0; while (__any(LD32(L_FB + (lane & 1) * 4) == SENT) && ++sp < LDS_CAP) __builtin_amdgcn_s_sleep(1); asm volatile("" ::: "memory"); }
      u32x4 bf[4];
#pragma unroll
      for (int ct = 0; ct < 4; ++ct) {
        LAS char* p = L + L_WO + (32 * t + 8 * c + (n >> 2)) * 128 + ct * 32 + (n & 3) * 8;
        const u32x2 lo = trd(p), hi = trd(p + 512);
        bf[ct] = u32x4{lo.x, lo.y, hi.x, hi.y};
      }
      asm volatile("s_waitcnt vmcnt(0) lgkmcnt(0)" : "+v"(a0), "+v"(bf[0]), "+v"(bf[1]), "+v"(bf[2]), "+v"(bf[3]) : "v"(oh) : "memory");
      const f32x2 xf2 = {bflo(xw), bfhi(xw)};
      u32 spins = 0;
      while (has_sent(a0) && ++spins < SPIN_CAP)
        asm volatile("s_sleep 2\n\tglobal_load_dwordx4 %0, %1, %2 sc1\n\ts_waitcnt vmcnt(0)" : "=&v"(a0) : "v"(o), "s"(ws) : "memory");
      f32x4 d[4];
#pragma unroll
      for (int ct = 0; ct < 4; ++ct) d[ct] = mfma(a0, bf[ct], f32x4{0.f, 0.f, 0.f, 0.f});
      __builtin_amdgcn_sched_barrier(0);
      f32x4 wt;
      {
        u32 f; spins = 0;
        do { f = LD32(L_FB + 12); wt = *(volatile LAS f32x4*)(L + L_WT + (t >> 2) * 64 + c * 16); }
        while (__builtin_amdgcn_readfirstlane(f) == SENT && ++spins < LDS_CAP);
      }
      float y[4];
#pragma unroll
      for (int ct = 0; ct < 4; ++ct) {
        const f32x2 e = f32x2{d[ct][0], d[ct][1]} * f32x2{wt[0], wt[1]} + f32x2{d[ct][2], d[ct][3]} * f32x2{wt[2], wt[3]};
        y[ct] = e[0] + e[1];
      }
      *(LAS float*)(L + L_RED + t * 256 + lane * 4) = sw16_add(sw32_add(y[0], y[2]), sw32_add(y[1], y[3]));
      BARRIER();
      if (t < 4) {
        f32x2 s[8];
#pragma unroll
        for (int k = 0; k < 8; ++k) s[k] = *(LAS f32x2*)(L + L_RED + k * 256 + (lane & 31) * 8);
        const f32x2 sum = (((s[0] + s[1]) + (s[2] + s[3])) + ((s[4] + s[5]) + (s[6] + s[7]))) + xf2;
        st4(ws, oh, nosent(cvtpk(sum.x, sum.y)));
      }
    }
    } else {
      {
        NOW_CLK();
        WAIT_CLK(R_CA);
        u32x2 cf; u32 sp = 0;
        do {
          asm volatile("s_sleep 4\n\tglobal_load_dwordx2 %0, %1, %2 sc1\n\ts_waitcnt vmcnt(0)" : "=v"(cf) : "v"(WS_CF + (g * 16 + n) * 8), "s"(ws) : "memory");
        } while (((cf.x ^ tag) | (cf.y ^ ~tag)) != 0u && ++sp < SPIN_CAP);
        SETF(L_FG);
      }
      { u32 sp = 0; while (__any(LD32(L_FP + (lane & 3) * 4) == SENT) && ++sp < LDS_CAP) __builtin_amdgcn_s_sleep(1); asm volatile("" ::: "memory"); }
      const u32x4 ll = LD128(L_LL + (lane & 3) * 16), mm = LD128(L_MM + (lane & 3) * 16);
      const float M = fmaxf(fmaxf(__uint_as_float(mm.x), __uint_as_float(mm.y)), fmaxf(__uint_as_float(mm.z), __uint_as_float(mm.w)));
      const float Ls = (__uint_as_float(ll.x) + __uint_as_float(ll.y)) + (__uint_as_float(ll.z) + __uint_as_float(ll.w));
      if (lane < 4) st8(ws, WS_LSE + (g * 16 + j) * 128 + lane * 8, u32x2{__float_as_uint(fmaf(M, 1.4426950408889634f, __builtin_amdgcn_logf(Ls))), tag});
      { u32 sp = 0; while (__any((int)LD32(L_PV + (lane & 1) * 4) < 8) && ++sp < LDS_CAP) __builtin_amdgcn_s_sleep(1); asm volatile("" ::: "memory"); }
      __builtin_amdgcn_s_sleep(LDL);
      u32 spins;
      u32x2 lv;
      const u32 lo = WS_LSE + (g * 16 + n) * 128 + (hp * 2 + (c & 1)) * 8;
      asm volatile("global_load_dwordx2 %0, %1, %2 sc1\n\ts_waitcnt vmcnt(0)" : "=v"(lv) : "v"(lo), "s"(ws) : "memory");
      spins = 0;
      while (lv.y != tag && ++spins < SPIN_CAP)
        asm volatile("s_sleep 4\n\tglobal_load_dwordx2 %0, %1, %2 sc1\n\ts_waitcnt vmcnt(0)" : "=v"(lv) : "v"(lo), "s"(ws) : "memory");
      const float ls = __uint_as_float(lv.x);
      float m = fmaxf(ls, dpp<XOR1>(ls)); m = fmaxf(m, dpp<XOR2>(m)); m = fmaxf(m, dpp<ROR4>(m)); m = fmaxf(m, dpp<ROR8>(m));
      const float e = __builtin_amdgcn_exp2f(ls - m);
      const float wgt = e * __builtin_amdgcn_rcpf(row16_sum(e));
      if (lane < 32) *(volatile LAS float*)(L + L_WT + lane * 4) = wgt;
      SETF(L_FB + 12);
    }
  } else if (w >= 3) {
    {
      asm volatile("s_waitcnt vmcnt(4)" : "+v"(q_[4]), "+v"(q_[5]) : "v"(codepf) : "memory");
      const u32x4 xa = q_[4];
      float xf[8] = {bflo(xa.x), bfhi(xa.x), bflo(xa.y), bfhi(xa.y), bflo(xa.z), bfhi(xa.z), bflo(xa.w), bfhi(xa.w)};
      const float sh = __builtin_bit_cast(float, __builtin_amdgcn_readfirstlane(__builtin_bit_cast(int, xf[0])));
      float s1 = 0.f, s2 = 0.f;
#pragma unroll
      for (int k = 0; k < 8; ++k) { const float d = xf[k] - sh; s1 += d; s2 = fmaf(d, d, s2); }
      s1 = wave_sum(s1) * (1.0f / 512); s2 = wave_sum(s2) * (1.0f / 512);
      const float mb = rbf(sh + s1), var = fmaxf(fmaf(-s1, s1, s2), 0.f);
      const float rden = __builtin_amdgcn_rcpf(rbf(__builtin_amdgcn_sqrtf(rbf(rbf(var) + 1e-5f))));
      const u32 xr0 = (rg & 1) ? q_[5].z : q_[5].x, xr1 = (rg & 1) ? q_[5].w : q_[5].y;
      u32 p01 = cvtpk(rbf(bflo(xr0) - mb) * rden, rbf(bfhi(xr0) - mb) * rden);
      u32 p23 = cvtpk(rbf(bflo(xr1) - mb) * rden, rbf(bfhi(xr1) - mb) * rden);
      asm volatile("s_waitcnt vmcnt(0)" : "+v"(q_[0]), "+v"(q_[1]), "+v"(q_[2]), "+v"(q_[3]), "+v"(p01), "+v"(p23) :: "memory");
      float acc[8];
#pragma unroll
      for (int d = 0; d < 4; ++d) {
        acc[2 * d] = dot2(permlo(q_[3][d], q_[2][d]), p23, dot2(permlo(q_[1][d], q_[0][d]), p01, 0.f));
        acc[2 * d + 1] = dot2(permhi(q_[3][d], q_[2][d]), p23, dot2(permhi(q_[1][d], q_[0][d]), p01, 0.f));
      }
      const float qp = treduce8(acc, lane);
      LD32(L_QP + t * 256 + (ch * 8 + rg) * 4) = nosent(__float_as_uint(qp));
      READ_TAG();
      if (t == 0) {
        u32x4 a, bb; u32 spins = 0;
        do {
#pragma unroll
          for (int k = 0; k < 4; ++k) { a[k] = LD32(L_QP + k * 256 + lane * 4); bb[k] = LD32(L_QP + 1024 + k * 256 + lane * 4); }
        } while ((has_sent(a) | has_sent(bb)) && ++spins < LDS_CAP);
        const f32x4 s = __builtin_bit_cast(f32x4, a) + __builtin_bit_cast(f32x4, bb);
        st8(ws, WS_QG + g * 8192 + hp * 4096 + (cb * 64 + lane) * 8, u32x2{__float_as_uint((s[0] + s[1]) + (s[2] + s[3])), tag});
      }
    }
    const int col0 = (2 * g + hp) * 32 + cb * 4;
    const float xv = __uint_as_float((u32)(*(const GAS u16*)(x + (col0 + c) * 2)) << 16);
    u32x4 a0, a1;
    { u32 sp = 0; while (__any(LD32(L_FG + (lane & 1) * 4) == SENT) && ++sp < LDS_CAP) __builtin_amdgcn_s_sleep(1); asm volatile("" ::: "memory"); }
    const int tb = (t + g + 1) & 7;
    const u32 o3 = WS_H3 + g * 16384 + n * 1024 + tb * 128 + c * 16;
    if (is_ffn) {
      u32x4 b1[2], b3[2], w2f[4];
#pragma unroll
      for (int s = 0; s < 2; ++s) {
        LAS char* p = L + L_W1 + (64 * tb + 32 * s + 8 * c + (n >> 2)) * 32 + (n & 3) * 8;
        u32x2 lo = trd(p), hi = trd(p + 128);
        b1[s] = u32x4{lo.x, lo.y, hi.x, hi.y};
        lo = trd(p + 16384); hi = trd(p + 16384 + 128);
        b3[s] = u32x4{lo.x, lo.y, hi.x, hi.y};
      }
#pragma unroll
      for (int k = 0; k < 2; ++k) {
        LAS char* p = L + L_W2 + (n >> 2) * 1024 + ((t & 3) * 128 + k * 64 + c * 16 + (n & 3) * 4) * 2;
        const u32x2 r0 = trd(p), r1 = trd(p + 4096), r2 = trd(p + 8192), r3 = trd(p + 12288);
        w2f[2 * k] = u32x4{r0.x, r0.y, r1.x, r1.y}; w2f[2 * k + 1] = u32x4{r2.x, r2.y, r3.x, r3.y};
      }
      const u32 o5 = WS_H5 + ((4 * t + (lane >> 5)) * 112 + u) * 128 + (lane & 31) * 4;
      u32x4 ones = {0x3F803F80u, 0x3F803F80u, 0x3F803F80u, 0x3F803F80u};
      {
        const f32x4 cg = mfma(ones, b1[1], mfma(ones, b1[0], f32x4{0.f, 0.f, 0.f, 0.f})), cu = mfma(ones, b3[1], mfma(ones, b3[0], f32x4{0.f, 0.f, 0.f, 0.f}));
        *(LAS float*)(L + L_RED + 2048 + tb * 512 + lane * 8 + 4) = (c & 1) ? cu[0] : cg[0];
      }
      asm volatile("s_waitcnt lgkmcnt(0)" : "+v"(w2f[0]), "+v"(w2f[1]), "+v"(w2f[2]), "+v"(w2f[3]), "+v"(ones) : "v"(o5) : "memory");
      WAITF(L_FB + 12, 1);
      gather2(ws, o3, o3 + 64, a0, a1);
      f32x4 dg = mfma(a0, b1[0], f32x4{0.f, 0.f, 0.f, 0.f}), du = mfma(a0, b3[0], f32x4{0.f, 0.f, 0.f, 0.f});
      f32x4 dq = mfma(a0, a0, f32x4{0.f, 0.f, 0.f, 0.f}), ds = mfma(a0, ones, f32x4{0.f, 0.f, 0.f, 0.f});
      dg = mfma(a1, b1[1], dg); du = mfma(a1, b3[1], du); dq = mfma(a1, a1, dq); ds = mfma(a1, ones, ds);
      __builtin_amdgcn_sched_barrier(0);
      *(LAS float*)(L + L_RED + 2048 + tb * 512 + lane * 8) = sw16_add(sw32_add(sum4(dg), sum4(dq)), sw32_add(sum4(du), sum4(ds)));
      BARRIER();
      if (t < 4) {
        f32x2 s[8];
#pragma unroll
        for (int k = 0; k < 8; ++k) s[k] = *(LAS f32x2*)(L + L_RED + 2048 + k * 512 + lane * 8);
        const f32x2 r = ((s[0] + s[1]) + (s[2] + s[3])) + ((s[4] + s[5]) + (s[6] + s[7]));
        const float mean = __builtin_bit_cast(float, __builtin_amdgcn_readlane(__builtin_bit_cast(int, r.x), 48)) * (1.0f / 512);
        const float sq = __builtin_bit_cast(float, __builtin_amdgcn_readlane(__builtin_bit_cast(int, row16_sum(r.x)), 32)) * (1.0f / 512);
        const float var = fmaxf(fmaf(-mean, mean, sq), 0.f);
        const float rden = __builtin_amdgcn_rcpf(rbf(__builtin_amdgcn_sqrtf(rbf(rbf(var) + 1e-5f))));
        const float v = rbf(fmaf(-rbf(mean), r.y, r.x) * rden);
        auto gu = __builtin_amdgcn_permlane16_swap(__float_as_uint(v), __float_as_uint(v), false, false);
        const float gt = __uint_as_float(gu[0]), up = __uint_as_float(gu[1]);
        const float sl = rbf(gt * __builtin_amdgcn_rcpf(1.0f + __builtin_amdgcn_exp2f(gt * -1.4426950408889634f)));
        const float act = sl * up;
        const u32 ap = cvtpk(act, dpp<XOR1>(act));
        u32 sa[8];
#pragma unroll
        for (int k = 0; k < 8; ++k) sa[k] = __builtin_amdgcn_readlane(ap, 2 * k);
        __builtin_amdgcn_sched_barrier(0);
        float y0 = 0.f, y1 = 0.f, y2 = 0.f, y3 = 0.f;
#pragma unroll
        for (int k = 0; k < 4; ++k) { y0 = dot2(w2f[0][k], sa[k], y0); y1 = dot2(w2f[1][k], sa[4 + k], y1); y2 = dot2(w2f[2][k], sa[k], y2); y3 = dot2(w2f[3][k], sa[4 + k], y3); }
        st4(ws, o5, nosent(__float_as_uint(y0 + y1)));
        st4(ws, o5 + 2 * 112 * 128, nosent(__float_as_uint(y2 + y3)));
      }
    }
    if (t == 7) {
      if (!is_ffn) { WAITF(L_FB + 12, 1); gather2(ws, o3, o3 + 64, a0, a1); __builtin_amdgcn_s_sleep(8); }
      __builtin_amdgcn_s_sleep(H5DELAY);
      const u32x4 ah = hp ? a1 : a0;
      const u32 e0 = (cb & 1) ? ah.z : ah.x, e1 = (cb & 1) ? ah.w : ah.y;
      const int src = (16 * (cb >> 1) + n) * 4;
      const u32 hw = __builtin_amdgcn_ds_bpermute(src, e0), hw1 = __builtin_amdgcn_ds_bpermute(src, e1);
      const u32 hsel = (c & 2) ? hw1 : hw;
      float h = rbf(xv + rbf(row16_sum((c & 1) ? bfhi(hsel) : bflo(hsel)) - xv));
      const u32 o5 = WS_H5 + (2 * g + hp) * (112 * 128) + cb * 16;
      asm volatile("" : "+v"(h));
      gather2(ws, o5 + lane * 128, o5 + min(64 + lane, 111) * 128, a0, a1);
      f32x4 s = __builtin_bit_cast(f32x4, a0);
      if (lane < 48) s += __builtin_bit_cast(f32x4, a1);
      const float tot = row16_sum(sw16_add(sw32_add(s[0], s[2]), sw32_add(s[1], s[3])));
      if (n == 0) asm volatile("global_store_short %0, %1, %2" :: "v"((col0 + c) * 2), "v"(cvtpk(h + rbf(tot), 0.f)), "s"(out_) : "memory");
    }
    if (b == 8 && tid == 64 * 9) st4(ws, WS_EPOCH, tag);
  } else if (w == 2) {
    u32 spins = 0;
    {
      NOW_CLK();
      WAIT_CLK(R_C);
      u32x4 cf;
      do {
        asm volatile("s_sleep 8\n\tglobal_load_dwordx4 %0, %1, %2 sc1\n\ts_waitcnt vmcnt(0)" : "=v"(cf) : "v"(WS_CF + lane * 16), "s"(ws) : "memory");
      } while ((((cf.x ^ tag) | (cf.z ^ tag)) | ((cf.y ^ ~tag) | (cf.w ^ ~tag))) != 0u && ++spins < SPIN_CAP);
      SETF(L_FG);
    }
    WAIT_CLK(R_L);
    SETF(L_FB + 12);
  } else if (w == 0) {
    asm volatile("s_waitcnt vmcnt(0)" :: "v"(codepf) : "memory");
    WAITF(L_TG, 1);
    NOW_CLK();
    WAIT_CLK(R_W);
    const u32 on = (u32)(lane >> 1) * (FF * 2) + u * 32 + (lane & 1) * 16;
    if (is_ffn) {
#pragma unroll
      for (u32 k = 0; k < 4; ++k) { const u32 kk = k + 4 * (u & 3), oo = on + kk * (32 * FF * 2); GLD("3", " nt", w1_, oo, L_W1 + kk * 1024); GLD("3", " nt", w3_, oo, L_W1 + 16384 + kk * 1024); }
      u32 o = u * 16384 + lane * 16;
#pragma unroll 4
      for (u32 k = L_W2; k < L_W2 + 16384; k += 1024) { GLD("15", " nt", w2_, o, k); o += 1024; }
#pragma unroll 2
      for (u32 k = 4; k < 16; ++k) {
        const u32 kk = (k + 4 * (u & 3)) & 15, oo = on + kk * (32 * FF * 2);
        GLD("15", "", w1_, oo, L_W1 + kk * 1024); GLD("15", "", w3_, oo, L_W1 + 16384 + kk * 1024);
      }
    }
    asm volatile("s_waitcnt vmcnt(0)" ::: "memory");
    SETF(L_FG + 4);
  } else {
    asm volatile("s_waitcnt vmcnt(0)" :: "v"(codepf) : "memory");
  }
}

extern "C" void kernel_launch(void* const* d_in, const int* in_sizes, int n_in,
                              void* d_out, int out_size, void* d_ws, size_t ws_size,
                              hipStream_t stream) {
  fused_decode<<<256, NTHR, 0, stream>>>((const char*)d_in[0], (const char*)d_in[1], (const char*)d_in[2], (const char*)d_in[3],
                                         (const char*)d_in[4], (const char*)d_in[6], (const char*)d_in[7], (const char*)d_in[8],
                                         (char*)d_out, (char*)d_ws);
}
```
